```python
import torch

def _softmax(x, axis=-1):
    return torch.softmax(x, dim=axis)
def _gelu(x):
    return torch.nn.functional.gelu(x, approximate="tanh")
def _silu(x):
    return torch.nn.functional.silu(x)
def _sigmoid(x):
    return torch.sigmoid(x)
def _layernorm(x, axis=-1, eps=1e-5):
    m = x.mean(dim=axis, keepdim=True)
    v = x.var(dim=axis, keepdim=True, unbiased=False)
    return (x - m) / torch.sqrt(v + eps)
def _scatter_add(tgt, idx, src, axis=0):
    return tgt.clone().index_add_(axis, idx, src)
def _segment_sum(src, seg, n):
    out = torch.zeros((n,) + src.shape[1:], dtype=src.dtype)
    return out.index_add_(0, seg, src)
def _pad(x, pw):
    flat = tuple(v for pair in reversed(pw) for v in pair)
    return torch.nn.functional.pad(x, flat)

kernel_name = 'gated_mix__softmax_full_mha__swiglu_ffn__prompt'

def setup_inputs():

    _w = (lambda a: (lambda t: t.to(torch.bfloat16) if t.is_floating_point() else t)(torch.as_tensor(a)))
    def _A_setup():
        B, S, D = 1, 2048, 512
        return {
            "x": _w((lambda *s: torch.randn(*s, dtype=torch.bfloat16) * 0.05)(B, S, D)),
            "wqkv": _w((lambda *s: torch.randn(*s, dtype=torch.bfloat16) * 0.8)(D, 3 * D)),
            "wo": _w((lambda *s: torch.randn(*s, dtype=torch.bfloat16) * 0.05)(D, D)),
        }
    _A_in = _A_setup()

    def _B_setup():
        B, S, D, F = 1, 2048, 512, 1376
        return {
            "x": _w((lambda *s: torch.randn(*s, dtype=torch.bfloat16) * 0.05)(B, S, D)),
            "w_gate": _w((lambda *s: torch.randn(*s, dtype=torch.bfloat16) * 0.1)(D, F)),
            "w_up": _w((lambda *s: torch.randn(*s, dtype=torch.bfloat16) * 0.1)(D, F)),
            "w_down": _w((lambda *s: torch.randn(*s, dtype=torch.bfloat16) * 0.1)(F, D)),
        }
    _B_in = _B_setup()

    return {
        "A_x": _A_in["x"],
        "A_wqkv": _A_in["wqkv"],
        "A_wo": _A_in["wo"],
        "B_x": _B_in["x"],
        "B_w_gate": _B_in["w_gate"],
        "B_w_up": _B_in["w_up"],
        "B_w_down": _B_in["w_down"],
        "gate_w": _w((lambda *s: torch.randn(*s, dtype=torch.bfloat16) * 0.05)(512) * 16.0),
    }

def reference(A_x, A_wqkv, A_wo, B_x, B_w_gate, B_w_up, B_w_down, gate_w):
    def _A_ref(x, wqkv, wo):
        B, S, D = x.shape
        H = 32; Dh = D // H
        qkv = (lambda x, *d: x.permute(*d))((x @ wqkv).reshape(B, S, 3, H, Dh), 2, 0, 3, 1, 4)
        q, k, v = qkv[0], qkv[1], qkv[2]
        scale = Dh ** -0.5
        scores = torch.einsum("bhqd,bhkd->bhqk", q, k) * scale
        p = _softmax(scores, axis=-1)
        o = torch.einsum("bhqk,bhkd->bhqd", p, v)
        return (lambda x, *d: x.permute(*d))(o, 0, 2, 1, 3).reshape(B, S, D) @ wo

    def _B_ref(x, w_gate, w_up, w_down):
        g = _silu(x @ w_gate)
        u = x @ w_up
        return (g * u) @ w_down

    x = A_x
    g = _sigmoid((x * gate_w).sum(-1, keepdims=True))
    a = _A_ref(x, A_wqkv, A_wo)
    b = _B_ref(x, B_w_gate, B_w_up, B_w_down)
    return g * a + (1 - g) * b
```

```cpp
#include <hip/hip_runtime.h>
#include <hip/hip_bf16.h>
#include <stdint.h>

typedef unsigned short u16;
typedef __attribute__((ext_vector_type(8))) __bf16 bf16x8;
typedef __attribute__((ext_vector_type(2))) __bf16 bf16x2;
typedef __attribute__((ext_vector_type(4))) short s16x4;
typedef __attribute__((ext_vector_type(16))) float f32x16;
typedef __attribute__((ext_vector_type(4))) float f32x4;
typedef __attribute__((ext_vector_type(4))) unsigned u32x4;
typedef __attribute__((ext_vector_type(2))) unsigned u32x2;
#define LDSP __attribute__((address_space(3)))
typedef LDSP char* ldsptr;

constexpr int S_ = 2048, D_ = 512, F_ = 1376, NH = 32;
constexpr int NTT = S_ / 32;
constexpr int NKF = F_ / 16;
constexpr float QSCALE = 0.25f * 1.4426950408889634f;

constexpr size_t WS_Q = 0;
constexpr size_t WS_K = WS_Q + (size_t)NH * NTT * 1024;
constexpr size_t WS_V = WS_K + (size_t)NH * NTT * 1024;
constexpr size_t WS_X = WS_V + (size_t)NH * NTT * 1024 + 4096;
constexpr size_t WS_O = WS_X + (size_t)NTT * 32 * 1024;
constexpr size_t WS_H = WS_O + (size_t)NTT * 32 * 1024;
constexpr size_t WS_B = WS_H + (size_t)NTT * NKF * 1024;
constexpr size_t WS_G = WS_B + (size_t)NTT * 16 * 2048;

#define MFMA(a, b, c) __builtin_amdgcn_mfma_f32_32x32x16_bf16((a), (b), (c), 0, 0, 0)
#define SBAR() __builtin_amdgcn_sched_barrier(0)

__device__ __forceinline__ unsigned cvtpk(float lo, float hi) {
    typedef __attribute__((ext_vector_type(2))) float f32x2;
    f32x2 v = {lo, hi};
    return __builtin_bit_cast(unsigned, __builtin_convertvector(v, bf16x2));
}
__device__ __forceinline__ void glds16(const void* sbase, unsigned voff, unsigned lds_base) {
    unsigned sv;
    asm volatile("s_mov_b32 %0, m0\n\ts_mov_b32 m0, %3\n\ts_nop 0\n\tglobal_load_lds_dwordx4 %1, %2\n\ts_mov_b32 m0, %0"
                 : "=&s"(sv) : "v"(voff), "s"(sbase), "s"(lds_base) : "memory");
}
__device__ __forceinline__ bf16x8 lds_b128(ldsptr p) { return *(LDSP bf16x8*)p; }
__device__ __forceinline__ s16x4 lds_tr(ldsptr p) {
    return __builtin_amdgcn_ds_read_tr16_b64_v4i16((LDSP s16x4*)p);
}
__device__ __forceinline__ bf16x8 tr_frag(ldsptr p0, ldsptr p1) {
    s16x4 a = lds_tr(p0), b = lds_tr(p1);
    typedef __attribute__((ext_vector_type(8))) short s16x8;
    s16x8 v = {a[0], a[1], a[2], a[3], b[0], b[1], b[2], b[3]};
    return __builtin_bit_cast(bf16x8, v);
}
__device__ __forceinline__ bf16x8 pack8(const f32x16& a, int o, float sc) {
    u32x4 v = {cvtpk(a[o] * sc, a[o + 1] * sc), cvtpk(a[o + 2] * sc, a[o + 3] * sc),
               cvtpk(a[o + 4] * sc, a[o + 5] * sc), cvtpk(a[o + 6] * sc, a[o + 7] * sc)};
    return __builtin_bit_cast(bf16x8, v);
}
__device__ __forceinline__ bf16x8 pack8(const f32x16& a, int o) {
    u32x4 v = {cvtpk(a[o], a[o + 1]), cvtpk(a[o + 2], a[o + 3]), cvtpk(a[o + 4], a[o + 5]), cvtpk(a[o + 6], a[o + 7])};
    return __builtin_bit_cast(bf16x8, v);
}
#define WAITV(n) asm volatile("s_waitcnt vmcnt(" #n ")" ::: "memory")
template <int N> __device__ __forceinline__ void wait_vm() { asm volatile("s_waitcnt vmcnt(%0)" ::"i"(N) : "memory"); }
__device__ __forceinline__ void bar() { asm volatile("s_waitcnt lgkmcnt(0)\n\ts_barrier" ::: "memory"); }

constexpr int K1_STAGE = 16384 + 12288, K1_NS = 5, K1_LDS = K1_STAGE * K1_NS;
__global__ __launch_bounds__(256, 1) void k1_qkv(const u16* __restrict__ x, const u16* __restrict__ wqkv, char* __restrict__ ws) {
    extern __shared__ __attribute__((aligned(16))) char smem[];
    const int tid = threadIdx.x, lane = tid & 63;
    const int wid = __builtin_amdgcn_readfirstlane(tid >> 6);
    const int xcd = blockIdx.x & 7, idx = blockIdx.x >> 3;
    const int rb = (xcd >> 1) * 4 + (idx & 3), hp = (xcd & 1) * 8 + (idx >> 2);
    const unsigned lds0 = (unsigned)(uintptr_t)smem;
    unsigned xoff[4], woff[3];
#pragma unroll
    for (int p = 0; p < 4; ++p) {
        const int row = wid * 32 + p * 8 + (lane >> 3);
        const int c = (lane & 7) ^ ((row >> 1) & 7);
        xoff[p] = (unsigned)((rb * 128 + row) * 1024 + c * 16);
    }
#pragma unroll
    for (int p = 0; p < 3; ++p) {
        const int n = (wid * 3 + p) * 64 + lane, kr = n / 12, ch = n % 12;
        woff[p] = (unsigned)(kr * 3072 + (ch >> 2) * 1024 + hp * 64 + (ch & 3) * 16);
    }
    auto issue = [&](int st) {
        const unsigned base = lds0 + (st % K1_NS) * K1_STAGE;
        const char* xs = (const char*)x + st * 128;
        const char* wsr = (const char*)wqkv + st * 64 * 3072;
#pragma unroll
        for (int p = 0; p < 4; ++p) glds16(xs, xoff[p], base + (wid * 4 + p) * 1024);
#pragma unroll
        for (int p = 0; p < 3; ++p) glds16(wsr, woff[p], base + 16384 + (wid * 3 + p) * 1024);
    };
#pragma unroll
    for (int st = 0; st < 4; ++st) issue(st);
    const int r = lane & 31, h = lane >> 5;
    ldsptr xrd[4];
#pragma unroll
    for (int ks = 0; ks < 4; ++ks) xrd[ks] = (ldsptr)smem + (wid * 32 + r) * 128 + (((2 * ks + h) ^ ((r >> 1) & 7)) * 16);
    ldsptr wrd = (ldsptr)smem + 16384 + (8 * h + ((lane & 15) >> 2)) * 192 + ((lane >> 4) & 1) * 32 + (lane & 3) * 8;
    f32x16 aq = {}, ak = {}, av = {};
    const int T = rb * 4 + wid;
    char* xf = ws + WS_X + (size_t)T * 32768 + lane * 16;
#pragma unroll
    for (int kt = 0; kt < 8; ++kt) {
        if (kt <= 4) wait_vm<21>(); else if (kt == 5) wait_vm<14>(); else if (kt == 6) wait_vm<7>(); else wait_vm<0>();
        bar();
        if (kt + 4 < 8) issue(kt + 4);
        const int so = (kt % K1_NS) * K1_STAGE;
#pragma unroll
        for (int ks = 0; ks < 4; ++ks) {
            bf16x8 xa = lds_b128(xrd[ks] + so);
            bf16x8 wq = tr_frag(wrd + so + ks * 16 * 192, wrd + so + ks * 16 * 192 + 4 * 192);
            bf16x8 wk = tr_frag(wrd + so + ks * 16 * 192 + 64, wrd + so + ks * 16 * 192 + 4 * 192 + 64);
            bf16x8 wv = tr_frag(wrd + so + ks * 16 * 192 + 128, wrd + so + ks * 16 * 192 + 4 * 192 + 128);
            aq = MFMA(wq, xa, aq);
            ak = MFMA(wk, xa, ak);
            av = MFMA(xa, wv, av);
            if (((kt * 4 + ks) >> 1) == hp) *(bf16x8*)(xf + (kt * 4 + ks) * 1024) = xa;
        }
    }
    char* qp = ws + WS_Q + ((size_t)(2 * hp) * NTT + T) * 1024 + lane * 16;
    char* kp = ws + WS_K + ((size_t)(2 * hp) * NTT + T) * 1024 + lane * 16;
    *(bf16x8*)qp = pack8(aq, 0, QSCALE);
    *(bf16x8*)(qp + NTT * 1024) = pack8(aq, 8, QSCALE);
    *(bf16x8*)kp = pack8(ak, 0);
    *(bf16x8*)(kp + NTT * 1024) = pack8(ak, 8);
    char* vp = ws + WS_V + ((size_t)(2 * hp + ((lane >> 4) & 1)) * NTT + T) * 1024 + h * 256 + (lane & 15) * 16;
    *(bf16x8*)vp = pack8(av, 0);
    *(bf16x8*)(vp + 512) = pack8(av, 8);
}

constexpr int F1_STAGE = 2 * 6144 + 64, F1_NS = 4;
__device__ __forceinline__ void ffn1_body(int b, char* smem, const u16* __restrict__ wg, const u16* __restrict__ wu,
                                          const u16* __restrict__ gatew, char* __restrict__ ws) {
    const int tid = threadIdx.x, lane = tid & 63;
    const int wid = __builtin_amdgcn_readfirstlane(tid >> 6);
    const int xcd = b & 7, idx = b >> 3;
    const int rb = (xcd >> 1) * 4 + (idx & 3), gi = (xcd & 1) * 8 + (idx >> 2);
    const int u0 = (gi * 80) / 15;
    const unsigned lds0 = (unsigned)(uintptr_t)smem;
    unsigned woff[3];
#pragma unroll
    for (int p = 0; p < 3; ++p) {
        const int n = ((wid & 1) * 3 + p) * 64 + lane, kr = n / 12, ch = n % 12;
        woff[p] = (unsigned)(kr * (F_ * 2) + u0 * 32 + ch * 16);
    }
    const char* wsrc = (const char*)((wid >> 1) ? wu : wg);
    const unsigned wdst = lds0 + (wid >> 1) * (6144 + 32) + (wid & 1) * 3072;
    auto issue = [&](int st) {
#pragma unroll
        for (int p = 0; p < 3; ++p) glds16(wsrc + st * 32 * (F_ * 2), woff[p], wdst + (st % F1_NS) * F1_STAGE + p * 1024);
    };
    const int T = rb * 4 + wid;
    const char* xf = ws + WS_X + (size_t)T * 32768 + lane * 16;
    const int h = lane >> 5;
    ldsptr wrd = (ldsptr)smem + (8 * h + ((lane & 15) >> 2)) * 192 + ((lane >> 4) & 1) * (6144 + 32) + (lane & 3) * 8;
    f32x16 acc[6] = {};
    float gacc = 0.f;
    issue(0); issue(1); issue(2);
    bf16x8 xa[2], xn[2];
    xa[0] = *(const bf16x8*)(xf); xa[1] = *(const bf16x8*)(xf + 1024);
#pragma unroll 1
    for (int st4 = 0; st4 < 16; st4 += 4) {
#pragma unroll
        for (int s = 0; s < 4; ++s) {
            const int st = st4 + s;
            wait_vm<0>();
            bar();
            if (st + 3 < 16) issue(st + 3);
            if (st + 1 < 16) { xn[0] = *(const bf16x8*)(xf + (2 * st + 2) * 1024); xn[1] = *(const bf16x8*)(xf + (2 * st + 3) * 1024); }
#pragma unroll
            for (int ks = 0; ks < 2; ++ks) {
#pragma unroll
                for (int u = 0; u < 6; ++u) {
                    bf16x8 w = tr_frag(wrd + s * F1_STAGE + ks * 16 * 192 + u * 32, wrd + s * F1_STAGE + ks * 16 * 192 + 4 * 192 + u * 32);
                    acc[u] = MFMA(w, xa[ks], acc[u]);
                }
                if (gi == 0) {
                    bf16x8 gw = *(const bf16x8*)(gatew + (2 * st + ks) * 16 + 8 * h);
#pragma unroll
                    for (int j = 0; j < 8; ++j) gacc += (float)xa[ks][j] * (float)gw[j];
                }
            }
            xa[0] = xn[0]; xa[1] = xn[1];
        }
    }
    char* hp = ws + WS_H + ((size_t)T * NKF + u0) * 1024 + lane * 16;
#pragma unroll
    for (int u = 0; u < 6; ++u) {
        f32x16 t;
#pragma unroll
        for (int j = 0; j < 8; ++j) {
            const float g = acc[u][j];
            const float e = __builtin_amdgcn_exp2f(-1.4426950408889634f * g);
            t[j] = g * __builtin_amdgcn_rcpf(1.0f + e) * acc[u][8 + j];
        }
        *(bf16x8*)(hp + u * 1024) = pack8(t, 0);
    }
    if (gi == 0) {
        gacc += __shfl_xor(gacc, 32);
        const float e = __builtin_amdgcn_exp2f(-1.4426950408889634f * gacc);
        if (lane < 32) ((float*)(ws + WS_G))[T * 32 + lane] = __builtin_amdgcn_rcpf(1.0f + e);
    }
}
__global__ __launch_bounds__(256, 3) void k_ffn1(const u16* __restrict__ wg, const u16* __restrict__ wu, const u16* __restrict__ gatew, char* __restrict__ ws) {
    extern __shared__ __attribute__((aligned(16))) char smem[];
    ffn1_body(blockIdx.x, smem, wg, wu, gatew, ws);
}

constexpr int F2_NS = 6;
__device__ __forceinline__ void ffn2_body(int b, char* smem, const u16* __restrict__ wd, char* __restrict__ ws) {
    const int tid = threadIdx.x, lane = tid & 63;
    const int wid = __builtin_amdgcn_readfirstlane(tid >> 6);
    const int xcd = b & 7, idx = b >> 3;
    const int mb = xcd * 4 + (idx & 3), nb = idx >> 2;
    const unsigned lds0 = (unsigned)(uintptr_t)smem + wid * (F2_NS * 2048);
    const int k0 = (wid * NKF) >> 2, k1 = ((wid + 1) * NKF) >> 2;
    unsigned woff[2];
#pragma unroll
    for (int p = 0; p < 2; ++p) {
        const int row = p * 8 + (lane >> 3);
        const int c = (lane & 7) ^ (((row >> 1) & 1) << 2);
        woff[p] = (unsigned)(row * 1024 + nb * 128 + c * 16);
    }
    auto issue = [&](int k, int slot) {
#pragma unroll
        for (int p = 0; p < 2; ++p) glds16((const char*)wd + (size_t)k * 16384, woff[p], lds0 + slot * 2048 + p * 1024);
    };
    const int h = lane >> 5, q = (lane & 15) >> 2, p4 = lane & 3, blk = (lane >> 4) & 1;
    const int row0 = 4 * h + q;
    const int sw = (row0 >> 1) & 1;
    ldsptr wrd0 = (ldsptr)smem + wid * (F2_NS * 2048) + row0 * 128 + sw * 64 + (p4 & 1) * 32 + blk * 16 + (p4 >> 1) * 8;
    ldsptr wrd1 = (ldsptr)smem + wid * (F2_NS * 2048) + row0 * 128 + (sw ^ 1) * 64 + (p4 & 1) * 32 + blk * 16 + (p4 >> 1) * 8;
    const char* hf0 = ws + WS_H + (size_t)(2 * mb) * NKF * 1024 + lane * 16;
    const char* hf1 = hf0 + NKF * 1024;
    f32x16 acc[4] = {};
#pragma unroll 1
    for (int kb = k0; kb < k1; kb += F2_NS) {
        bf16x8 ha[F2_NS], hb[F2_NS];
#pragma unroll
        for (int j = 0; j < F2_NS; ++j) {
            const int k = min(kb + j, k1 - 1);
            issue(k, j);
            ha[j] = *(const bf16x8*)(hf0 + k * 1024);
            hb[j] = *(const bf16x8*)(hf1 + k * 1024);
        }
        wait_vm<0>();
#pragma unroll
        for (int j = 0; j < F2_NS; ++j) {
            if (kb + j < k1) {
                bf16x8 w0 = tr_frag(wrd0 + j * 2048, wrd0 + j * 2048 + 1024);
                bf16x8 w1 = tr_frag(wrd1 + j * 2048, wrd1 + j * 2048 + 1024);
                acc[0] = MFMA(w0, ha[j], acc[0]);
                acc[1] = MFMA(w1, ha[j], acc[1]);
                acc[2] = MFMA(w0, hb[j], acc[2]);
                acc[3] = MFMA(w1, hb[j], acc[3]);
            }
        }
        asm volatile("s_waitcnt lgkmcnt(0)" ::: "memory");
    }
    bar();
    LDSP f32x4* red = (LDSP f32x4*)smem;
#pragma unroll
    for (int t = 0; t < 4; ++t) {
        if (t != wid) {
            const int slot = (wid - t - 1) & 3;
#pragma unroll
            for (int i = 0; i < 4; ++i) {
                f32x4 v = {acc[t][4 * i], acc[t][4 * i + 1], acc[t][4 * i + 2], acc[t][4 * i + 3]};
                red[((t * 3 + slot) * 4 + i) * 64 + lane] = v;
            }
        }
    }
    bar();
    f32x16 o = (wid == 0) ? acc[0] : (wid == 1) ? acc[1] : (wid == 2) ? acc[2] : acc[3];
#pragma unroll
    for (int s = 0; s < 3; ++s)
#pragma unroll
        for (int i = 0; i < 4; ++i) {
            f32x4 v = red[((wid * 3 + s) * 4 + i) * 64 + lane];
            o[4 * i] += v[0]; o[4 * i + 1] += v[1]; o[4 * i + 2] += v[2]; o[4 * i + 3] += v[3];
        }
    char* bp = ws + WS_B + ((size_t)(2 * mb + (wid >> 1)) * 16 + 2 * nb + (wid & 1)) * 2048 + lane * 16;
    *(bf16x8*)bp = pack8(o, 0);
    *(bf16x8*)(bp + 1024) = pack8(o, 8);
}
__global__ __launch_bounds__(256, 3) void k_ffn2(const u16* __restrict__ wd, char* __restrict__ ws) {
    extern __shared__ __attribute__((aligned(16))) char smem[];
    ffn2_body(blockIdx.x, smem, wd, ws);
}

constexpr int TPC = 4, NCH = NTT / TPC, A_NS = 6;
constexpr int A_SLOT = TPC * 1024 + TPC * 1040;
constexpr int A_LDS = A_SLOT * A_NS;

__device__ __forceinline__ f32x16 attn_fallback(const char* kf, const char* vf, bf16x8 qf, int lane) {
    f32x16 o = {};
    float m = -INFINITY;
    const bool isv = ((lane >> 4) & 1) == 0;
    const u32x4 ones = {0x3f803f80u, 0x3f803f80u, 0x3f803f80u, 0x3f803f80u};
    for (int t = 0; t < NTT; ++t) {
        bf16x8 kk = *(const bf16x8*)(kf + t * 1024 + lane * 16);
        const char* vp = vf + t * 1024 + (lane >> 5) * 256 + (lane & 15) * 16;
        u32x4 v0 = *(const u32x4*)vp, v1 = *(const u32x4*)(vp + 512);
        if (!isv) { v0 = ones; v1 = ones; }
        f32x16 z = {};
        f32x16 s = MFMA(kk, qf, z);
        float tm = s[0];
#pragma unroll
        for (int i = 1; i < 16; ++i) tm = fmaxf(tm, s[i]);
        tm = fmaxf(tm, __shfl_xor(tm, 32));
        const float mn = fmaxf(m, tm);
        const float al = __builtin_amdgcn_exp2f(m - mn);
        m = mn;
#pragma unroll
        for (int i = 0; i < 16; ++i) { o[i] *= al; s[i] = __builtin_amdgcn_exp2f(s[i] - mn); }
        o = MFMA(__builtin_bit_cast(bf16x8, v0), pack8(s, 0), o);
        o = MFMA(__builtin_bit_cast(bf16x8, v1), pack8(s, 8), o);
    }
    return o;
}

__device__ __forceinline__ void attn_body(int b, char* smem, char* __restrict__ ws) {
    const int tid = threadIdx.x, lane = tid & 63;
    const int wid = __builtin_amdgcn_readfirstlane(tid >> 6);
    const int xcd = b & 7, idx = b >> 3;
    const int head = xcd * 4 + (idx & 3), qb = idx >> 2;
    const int T = qb * 4 + wid;
    const unsigned lds0 = (unsigned)(uintptr_t)smem;
    const char* kf = ws + WS_K + (size_t)head * NTT * 1024;
    const char* vf = ws + WS_V + (size_t)head * NTT * 1024;
    const unsigned loff = lane * 16;
    auto issue = [&](int c) {
        const unsigned base = lds0 + (c % A_NS) * A_SLOT;
        glds16(kf + (c * TPC + 1 + wid) * 1024, loff, base + wid * 1024);
        glds16(vf + (c * TPC + wid) * 1024, loff, base + TPC * 1024 + wid * 1040);
    };
    const bf16x8 qf = *(const bf16x8*)(ws + WS_Q + ((size_t)head * NTT + T) * 1024 + loff);
    const bf16x8 k0 = *(const bf16x8*)(kf + loff);
#pragma unroll
    for (int c = 0; c < A_NS - 1; ++c) issue(c);
    if (tid < A_NS * TPC) {
        const u32x4 ones = {0x3f803f80u, 0x3f803f80u, 0x3f803f80u, 0x3f803f80u};
        *(LDSP u32x4*)((ldsptr)smem + (tid / TPC) * A_SLOT + TPC * 1024 + (tid % TPC) * 1040 + 1024) = ones;
    }
    const bool isv = ((lane >> 4) & 1) == 0;
    ldsptr krd = (ldsptr)smem + lane * 16;
    ldsptr vrd0 = (ldsptr)smem + TPC * 1024 + (isv ? (lane >> 5) * 256 + (lane & 15) * 16 : 1024);
    ldsptr vrd1 = (ldsptr)smem + TPC * 1024 + (isv ? 512 + (lane >> 5) * 256 + (lane & 15) * 16 : 1024);
    f32x16 z = {};
    f32x16 s = MFMA(k0, qf, z);
    float m = s[0];
#pragma unroll
    for (int i = 1; i < 16; ++i) m = fmaxf(m, s[i]);
    m = fmaxf(m, __shfl_xor(m, 32));
    f32x16 negm;
#pragma unroll
    for (int i = 0; i < 16; ++i) { negm[i] = -m; s[i] -= m; }
    f32x16 o = {};
    int slot = 0;
#pragma unroll 1
    for (int c = 0; c < NCH; ++c) {
        if (c + A_NS - 1 <= NCH) wait_vm<2 * (A_NS - 2)>(); else wait_vm<0>();
        bar();
        if (c + A_NS - 1 < NCH) issue(c + A_NS - 1);
        const int so = slot * A_SLOT;
#pragma unroll
        for (int i = 0; i < TPC; ++i) {
            bf16x8 kk = lds_b128(krd + so + i * 1024);
            bf16x8 v0 = lds_b128(vrd0 + so + i * 1040);
            bf16x8 v1 = lds_b128(vrd1 + so + i * 1040);
            f32x16 sn = MFMA(kk, qf, negm);
#pragma unroll
            for (int j = 0; j < 16; ++j) s[j] = __builtin_amdgcn_exp2f(s[j]);
            o = MFMA(v0, pack8(s, 0), o);
            o = MFMA(v1, pack8(s, 8), o);
            s = sn;
        }
        slot = (slot == A_NS - 1) ? 0 : slot + 1;
    }
    float chk = 0.f;
#pragma unroll
    for (int i = 0; i < 8; ++i) chk = fmaxf(chk, fabsf(o[i]));
    const bool bad = !(o[8] <= 1.8446744e19f) || !(chk <= 3.0e38f) || (o[0] != o[0]) || (o[1] != o[1]) || (o[2] != o[2]) || (o[3] != o[3]) ||
                     (o[4] != o[4]) || (o[5] != o[5]) || (o[6] != o[6]) || (o[7] != o[7]);
    if (__builtin_expect(__any(bad), 0)) o = attn_fallback(kf, vf, qf, lane);
    const float rl = __builtin_amdgcn_rcpf(o[8]);
    *(bf16x8*)(ws + WS_O + ((size_t)T * NH + head) * 1024 + loff) = pack8(o, 0, rl);
}
__global__ __launch_bounds__(256, 3) void k_attn(char* __restrict__ ws) {
    extern __shared__ __attribute__((aligned(16))) char smem[];
    attn_body(blockIdx.x, smem, ws);
}

constexpr int K3_LDS = 131072;
__global__ __launch_bounds__(256, 1) void k3_out(const u16* __restrict__ wo, const char* __restrict__ ws, u16* __restrict__ out) {
    extern __shared__ __attribute__((aligned(16))) char smem[];
    const int tid = threadIdx.x, lane = tid & 63;
    const int wid = __builtin_amdgcn_readfirstlane(tid >> 6);
    const int xcd = blockIdx.x & 7, idx = blockIdx.x >> 3;
    const int mb = xcd * 4 + (idx & 3), nb = idx >> 2;
    const unsigned lds0 = (unsigned)(uintptr_t)smem;
    unsigned woff;
    {
        const int row = (wid & 1) * 8 + (lane >> 3);
        const int c = (lane & 7) ^ (((row >> 1) & 1) << 2);
        woff = (unsigned)(row * 1024 + nb * 128 + c * 16);
    }
    const char* src = (wid < 2) ? ws + WS_O + (size_t)(2 * mb + wid) * NH * 1024 : (const char*)wo;
    const unsigned voff = (wid < 2) ? lane * 16 : woff;
    const int kstride = (wid < 2) ? 1024 : 16384;
    const int mt = wid >> 1, nt = wid & 1;
    const int T = 2 * mb + mt;
    const float g = ((const float*)(ws + WS_G))[T * 32 + (lane & 31)];
    const char* bp = ws + WS_B + ((size_t)T * 16 + 2 * nb + nt) * 2048 + lane * 16;
    const bf16x8 b0 = *(const bf16x8*)bp, b1 = *(const bf16x8*)(bp + 1024);
#pragma unroll
    for (int k = 0; k < 32; ++k) glds16(src + k * kstride, voff, lds0 + k * 4096 + wid * 1024);
    const int h = lane >> 5, q = (lane & 15) >> 2, p4 = lane & 3, blk = (lane >> 4) & 1;
    const int row0 = 4 * h + q;
    ldsptr wrd = (ldsptr)smem + 2048 + row0 * 128 + ((((row0 >> 1) & 1) ^ nt) * 64) + (p4 & 1) * 32 + blk * 16 + (p4 >> 1) * 8;
    ldsptr ord = (ldsptr)smem + mt * 1024 + lane * 16;
    f32x16 acc = {};
#pragma unroll
    for (int kq = 0; kq < 4; ++kq) {
        if (kq == 0) wait_vm<24>(); else if (kq == 1) wait_vm<16>(); else if (kq == 2) wait_vm<8>(); else wait_vm<0>();
        bar();
#pragma unroll
        for (int kk = 0; kk < 8; ++kk) {
            const int k = kq * 8 + kk;
            bf16x8 w = tr_frag(wrd + k * 4096, wrd + k * 4096 + 1024);
            bf16x8 oa = lds_b128(ord + k * 4096);
            acc = MFMA(w, oa, acc);
        }
    }
    const float g1 = 1.0f - g;
    f32x16 res;
#pragma unroll
    for (int i = 0; i < 8; ++i) { res[i] = g * acc[i] + g1 * (float)b0[i]; res[8 + i] = g * acc[8 + i] + g1 * (float)b1[i]; }
    char* op = (char*)out + (size_t)(T * 32 + (lane & 31)) * 1024 + (nb * 64 + nt * 32 + h * 16) * 2;
    *(bf16x8*)op = pack8(res, 0);
    *(bf16x8*)(op + 16) = pack8(res, 8);
}

extern "C" void kernel_launch(void* const* d_in, const int* in_sizes, int n_in, void* d_out, int out_size, void* d_ws,
                              size_t ws_size, hipStream_t stream) {
    const u16* x = (const u16*)d_in[0];
    const u16* wqkv = (const u16*)d_in[1];
    const u16* wo = (const u16*)d_in[2];
    const u16* wg = (const u16*)d_in[4];
    const u16* wu = (const u16*)d_in[5];
    const u16* wd = (const u16*)d_in[6];
    const u16* gw = (const u16*)d_in[7];
    char* ws = (char*)d_ws;
    (void)hipFuncSetAttribute((const void*)k1_qkv, hipFuncAttributeMaxDynamicSharedMemorySize, K1_LDS);
    (void)hipFuncSetAttribute((const void*)k3_out, hipFuncAttributeMaxDynamicSharedMemorySize, K3_LDS);
    k1_qkv<<<256, 256, K1_LDS, stream>>>(x, wqkv, ws);
    k_ffn1<<<256, 256, F1_STAGE * F1_NS, stream>>>(wg, wu, gw, ws);
    k_ffn2<<<256, 256, 4 * F2_NS * 2048, stream>>>(wd, ws);
    k_attn<<<512, 256, A_LDS, stream>>>(ws);
    k3_out<<<256, 256, K3_LDS, stream>>>(wo, ws, (u16*)d_out);
}
```

```cpp
#include <hip/hip_runtime.h>
#include <hip/hip_bf16.h>
#include <stdint.h>

#define FPRIO 3
#define AP0 2
#define AP1 1
#define AP2 0
#define SLP 4
typedef unsigned short u16;
typedef __attribute__((ext_vector_type(8))) __bf16 bf16x8;
typedef __attribute__((ext_vector_type(2))) __bf16 bf16x2;
typedef __attribute__((ext_vector_type(4))) short s16x4;
typedef __attribute__((ext_vector_type(16))) float f32x16;
typedef __attribute__((ext_vector_type(4))) float f32x4;
typedef __attribute__((ext_vector_type(2))) float f32x2;
typedef __attribute__((ext_vector_type(4))) unsigned u32x4;
#define LDSP __attribute__((address_space(3)))
typedef LDSP char* ldsptr;
typedef __attribute__((address_space(1))) unsigned gu32;

constexpr int S_ = 2048, D_ = 512, F_ = 1376, NH = 32;
constexpr int NTT = S_ / 32;
constexpr int NKF = F_ / 16;
constexpr float LOG2E = 1.4426950408889634f;
constexpr float QSCALE = 0.25f * LOG2E;

constexpr size_t WS_FLAG = 0;
constexpr size_t WS_Q = 4096;
constexpr size_t WS_K = WS_Q + (size_t)NH * NTT * 1024;
constexpr size_t WS_V = WS_K + (size_t)NH * NTT * 1024;
constexpr size_t WS_X = WS_V + (size_t)NH * NTT * 1024 + 8192;
constexpr size_t WS_O = WS_X + (size_t)NTT * 32 * 1024;
constexpr size_t WS_H = WS_O + (size_t)NTT * 32 * 1024;
constexpr size_t WS_G = WS_H + (size_t)NTT * NKF * 1024;

#define MFMA(a, b, c) __builtin_amdgcn_mfma_f32_32x32x16_bf16((a), (b), (c), 0, 0, 0)
#define SBAR() __builtin_amdgcn_sched_barrier(0)
#define EXP2(x) __builtin_amdgcn_exp2f(x)
#define STAMP(k, i)
#define STAMP0(k)
#define STAMPE(k, i)

__device__ __forceinline__ unsigned cvtpk(float lo, float hi) {
    f32x2 v = {lo, hi};
    return __builtin_bit_cast(unsigned, __builtin_convertvector(v, bf16x2));
}
__device__ __forceinline__ void glds16(const void* sbase, unsigned voff, unsigned lds_base) {
    unsigned sv;
    asm volatile("s_mov_b32 %0, m0\n\ts_mov_b32 m0, %3\n\ts_nop 0\n\tglobal_load_lds_dwordx4 %1, %2\n\ts_mov_b32 m0, %0"
                 : "=&s"(sv) : "v"(voff), "s"(sbase), "s"(lds_base) : "memory");
}
__device__ __forceinline__ void gld16(bf16x8& r, const void* sbase, unsigned voff) {
    asm volatile("global_load_dwordx4 %0, %1, %2" : "=v"(r) : "v"(voff), "s"(sbase) : "memory");
}
__device__ __forceinline__ void gld16_sc1(bf16x8& r, const void* sbase, unsigned voff) {
    asm volatile("global_load_dwordx4 %0, %1, %2 sc1" : "=v"(r) : "v"(voff), "s"(sbase) : "memory");
}
__device__ __forceinline__ void gst16_sc1(void* sbase, unsigned voff, bf16x8 v) {
    asm volatile("global_store_dwordx4 %0, %1, %2 sc1\n\ts_nop 0" :: "v"(voff), "v"(v), "s"(sbase) : "memory");
}
template <int N> __device__ __forceinline__ void wait_vm() { asm volatile("s_waitcnt vmcnt(%0)" ::"i"(N) : "memory"); }
template <int N> __device__ __forceinline__ void wait_vm2(bf16x8& a, bf16x8& b) {
    asm volatile("s_waitcnt vmcnt(%2)" : "+v"(a), "+v"(b) : "i"(N) : "memory");
}
__device__ __forceinline__ void bar() { asm volatile("s_waitcnt lgkmcnt(0)\n\ts_barrier" ::: "memory"); }
__device__ __forceinline__ bf16x8 lds_b128(ldsptr p) { return *(LDSP bf16x8*)p; }
__device__ __forceinline__ bf16x8 tr_frag(ldsptr p0, ldsptr p1) {
    s16x4 a = __builtin_amdgcn_ds_read_tr16_b64_v4i16((LDSP s16x4*)p0), b = __builtin_amdgcn_ds_read_tr16_b64_v4i16((LDSP s16x4*)p1);
    typedef __attribute__((ext_vector_type(8))) short s16x8;
    s16x8 v = {a[0], a[1], a[2], a[3], b[0], b[1], b[2], b[3]};
    return __builtin_bit_cast(bf16x8, v);
}
__device__ __forceinline__ bf16x8 pack8(const f32x16& a, int o, float sc) {
    u32x4 v = {cvtpk(a[o] * sc, a[o + 1] * sc), cvtpk(a[o + 2] * sc, a[o + 3] * sc),
               cvtpk(a[o + 4] * sc, a[o + 5] * sc), cvtpk(a[o + 6] * sc, a[o + 7] * sc)};
    return __builtin_bit_cast(bf16x8, v);
}
__device__ __forceinline__ bf16x8 pack8(const f32x16& a, int o) {
    u32x4 v = {cvtpk(a[o], a[o + 1]), cvtpk(a[o + 2], a[o + 3]), cvtpk(a[o + 4], a[o + 5]), cvtpk(a[o + 6], a[o + 7])};
    return __builtin_bit_cast(bf16x8, v);
}
__device__ __forceinline__ void wait_flags(const char* line, int lane, bool need) {
    gu32* p = (gu32*)(line) + (lane & 31);
    for (int spin = 0; spin < (1 << 22); ++spin) {
        const unsigned v = __hip_atomic_load(p, __ATOMIC_RELAXED, __HIP_MEMORY_SCOPE_AGENT);
        if (__all(!need || v != 0u)) break;
        __builtin_amdgcn_s_sleep(SLP);
    }
}

constexpr int K1_STAGE = 32768, K1_NS = 5, K1_LDS = K1_STAGE * K1_NS;
__global__ __launch_bounds__(512, 2) void k1_qkv(const u16* __restrict__ x, const u16* __restrict__ wqkv,
                                                 const u16* __restrict__ gatew, char* __restrict__ ws) {
    extern __shared__ __attribute__((aligned(16))) char smem[];
    const int tid = threadIdx.x, lane = tid & 63;
    const int wid = __builtin_amdgcn_readfirstlane(tid >> 6);
    STAMP0(0);
    const int xcd = blockIdx.x & 7, idx = blockIdx.x >> 3;
    const int rb = (xcd >> 1) * 8 + (idx & 7), cg = (xcd & 1) * 4 + (idx >> 3);
    const unsigned lds0 = (unsigned)(uintptr_t)smem;
    const int row = wid * 8 + (lane >> 3);
    const unsigned xoff = (unsigned)((rb * 64 + row) * 1024 + (((lane & 7) ^ ((row >> 1) & 7)) * 16));
    const unsigned woff = (unsigned)(row * 3072 + cg * 128 + (((lane & 7) ^ (((row >> 1) & 1) << 2)) * 16));
    auto issue = [&](int st, int dw) {
        const unsigned base = lds0 + (st % K1_NS) * K1_STAGE + (wid + dw) * 1024;
        const char* wsr = (const char*)wqkv + (st * 64 + dw * 8) * 3072;
        glds16((const char*)x + st * 128 + dw * 8192, xoff, base);
#pragma unroll
        for (int m = 0; m < 3; ++m) glds16(wsr + m * 1024, woff, base + 8192 + m * 8192);
    };
    const int r = lane & 31, h = lane >> 5;
    if (wid < 4) {
#pragma unroll
        for (int st = 0; st < K1_NS; ++st) issue(st, 0);
        const int mt = wid & 1, chf = wid >> 1;
        ldsptr xrd[4];
#pragma unroll
        for (int ks = 0; ks < 4; ++ks) xrd[ks] = (ldsptr)smem + (mt * 32 + r) * 128 + (((2 * ks + h) ^ ((r >> 1) & 7)) * 16);
        const int q = (lane & 15) >> 2;
        ldsptr wrd = (ldsptr)smem + 8192 + (8 * h + q) * 128 + ((chf ^ (q >> 1)) * 64) + ((lane >> 4) & 1) * 32 + (lane & 3) * 8;
        f32x16 aq = {}, ak = {}, av = {};
        bf16x8 xa, wq, wk, wv;
        auto rd = [&](int g, bf16x8& a, bf16x8& b0, bf16x8& b1, bf16x8& b2) {
            const int so = ((g >> 2) % K1_NS) * K1_STAGE, ks = g & 3;
            a = lds_b128(xrd[ks] + so);
            b0 = tr_frag(wrd + so + ks * 2048, wrd + so + ks * 2048 + 512);
            b1 = tr_frag(wrd + so + 8192 + ks * 2048, wrd + so + 8192 + ks * 2048 + 512);
            b2 = tr_frag(wrd + so + 16384 + ks * 2048, wrd + so + 16384 + ks * 2048 + 512);
        };
        wait_vm<16>();
        bar();
        STAMP(0, 1);
        rd(0, xa, wq, wk, wv);
#pragma unroll
        for (int g = 0; g < 32; ++g) {
            if ((g & 3) == 3 && g < 31) {
                const int kt = g >> 2;
                if (kt == 0) wait_vm<12>(); else if (kt == 1) wait_vm<8>(); else if (kt == 2) wait_vm<4>(); else if (kt == 3) wait_vm<0>();
                bar();
            }
            bf16x8 na = xa, n0 = wq, n1 = wk, n2 = wv;
            if (g < 31) rd(g + 1, na, n0, n1, n2);
            SBAR();
            aq = MFMA(wq, xa, aq);
            ak = MFMA(wk, xa, ak);
            av = MFMA(xa, wv, av);
            SBAR();
            xa = na; wq = n0; wk = n1; wv = n2;
        }
        STAMP(0, 2);
        const int hp = cg * 2 + chf, T = rb * 2 + mt;
        char* qp = ws + WS_Q + ((size_t)(2 * hp) * NTT + T) * 1024 + lane * 16;
        char* kp = ws + WS_K + ((size_t)(2 * hp) * NTT + T) * 1024 + lane * 16;
        *(bf16x8*)qp = pack8(aq, 0, QSCALE);
        *(bf16x8*)(qp + NTT * 1024) = pack8(aq, 8, QSCALE);
        *(bf16x8*)kp = pack8(ak, 0);
        *(bf16x8*)(kp + NTT * 1024) = pack8(ak, 8);
        char* vp = ws + WS_V + ((size_t)(2 * hp + ((lane >> 4) & 1)) * NTT + T) * 1024 + h * 256 + (lane & 15) * 16;
        *(bf16x8*)vp = pack8(av, 0);
        *(bf16x8*)(vp + 512) = pack8(av, 8);
        STAMPE(0, 3);
    } else {
        const int lw = wid - 4;
        const int gt = rb * 64 + cg * 8 + lw * 2 + h;
        const bf16x8 gx0 = *(const bf16x8*)(x + gt * 512 + r * 16), gx1 = *(const bf16x8*)(x + gt * 512 + r * 16 + 8);
        const bf16x8 gw0 = *(const bf16x8*)(gatew + r * 16), gw1 = *(const bf16x8*)(gatew + r * 16 + 8);
#pragma unroll
        for (int st = 0; st < K1_NS; ++st) issue(st, 0);
        if (blockIdx.x == 0) *(u32x4*)(ws + WS_FLAG + (tid - 256) * 16) = u32x4{0, 0, 0, 0};
        const int mt = lw & 1, k2 = (lw >> 1) * 2;
        ldsptr xr0 = (ldsptr)smem + (mt * 32 + r) * 128 + (((2 * k2 + h) ^ ((r >> 1) & 7)) * 16);
        ldsptr xr1 = (ldsptr)smem + (mt * 32 + r) * 128 + (((2 * k2 + 2 + h) ^ ((r >> 1) & 7)) * 16);
        char* xf = ws + WS_X + ((size_t)(rb * 2 + mt) * 32 + cg * 4 + k2) * 1024 + lane * 16;
#pragma unroll
        for (int j = 0; j < 8; ++j) {
            if (j == 0) wait_vm<16>(); else if (j == 1) wait_vm<12>(); else if (j == 2) wait_vm<16>(); else if (j == 3) wait_vm<20>();
            else if (j == 4) wait_vm<24>(); else if (j == 5) wait_vm<16>(); else if (j == 6) wait_vm<8>(); else wait_vm<0>();
            bar();
            if (j >= 1 && j <= 3) { issue(j + 4, 0); issue(j + 4, -4); }
            if (j == cg) {
                const int so = (j % K1_NS) * K1_STAGE;
                const bf16x8 a = lds_b128(xr0 + so), b = lds_b128(xr1 + so);
                *(bf16x8*)xf = a;
                *(bf16x8*)(xf + 1024) = b;
            }
        }
        float acc = 0.f;
#pragma unroll
        for (int j = 0; j < 8; ++j) acc += (float)gx0[j] * (float)gw0[j] + (float)gx1[j] * (float)gw1[j];
#pragma unroll
        for (int o = 16; o >= 1; o >>= 1) acc += __shfl_xor(acc, o);
        const float e = EXP2(-LOG2E * acc);
        if (r == 0) ((float*)(ws + WS_G))[gt] = __builtin_amdgcn_rcpf(1.0f + e);
    }
}

constexpr int F1_STAGE = 2 * 6144 + 64, F1_NS = 4;
constexpr int F2_NS = 6;
__device__ __forceinline__ void ffn_role(int b, char* smem, const u16* __restrict__ wg, const u16* __restrict__ wu,
                                         const u16* __restrict__ wd, const u16* __restrict__ wo, char* __restrict__ ws,
                                         u16* __restrict__ out) {
    const int tid = threadIdx.x, lane = tid & 63;
    const int wid = __builtin_amdgcn_readfirstlane(tid >> 6);
    const int xcd = b & 7, idx = b >> 3;
    const unsigned lds0 = (unsigned)(uintptr_t)smem;
    const unsigned loff = lane * 16;
    const int h = lane >> 5, q = (lane & 15) >> 2, p4 = lane & 3, blk = (lane >> 4) & 1;
    __builtin_amdgcn_s_setprio(FPRIO);
    STAMP0(1);
    const int mb = xcd * 4 + (idx & 3), nb = idx >> 2;
    const int k0 = wid * 21 + (wid < 2 ? wid : 2);
    const int klast = k0 + (wid < 2 ? 21 : 20);
    unsigned doff;
    {
        const int row = lane >> 3;
        doff = (unsigned)(row * 1024 + nb * 128 + (((lane & 7) ^ (((row >> 1) & 1) << 2)) * 16));
    }
    const unsigned dl0 = lds0 + wid * (F2_NS * 2048);
    auto issue_w = [&](const u16* w, int k, int slot) {
        glds16((const char*)w + (size_t)k * 16384, doff, dl0 + slot * 2048);
        glds16((const char*)w + (size_t)k * 16384 + 8192, doff, dl0 + slot * 2048 + 1024);
    };
    {
        const int rb = (xcd >> 1) * 4 + (idx & 3), gi = (xcd & 1) * 8 + (idx >> 2);
        if (gi < 15) {
            const int u0 = gi < 14 ? gi * 6 : 80;
            unsigned woff[3];
#pragma unroll
            for (int p = 0; p < 3; ++p) {
                const int n = ((wid & 1) * 3 + p) * 64 + lane, kr = n / 12, ch = n % 12;
                woff[p] = (unsigned)(kr * (F_ * 2) + u0 * 32 + ch * 16);
            }
            const char* wsrc = (const char*)((wid >> 1) ? wu : wg);
            const unsigned wdst = lds0 + (wid >> 1) * (6144 + 32) + (wid & 1) * 3072;
            const int T = rb * 4 + wid;
            const char* xf = ws + WS_X + (size_t)T * 32768;
            bf16x8 xr[4][2];
            auto issue = [&](int st, int s) {
#pragma unroll
                for (int p = 0; p < 3; ++p) glds16(wsrc + st * 32 * (F_ * 2), woff[p], wdst + s * F1_STAGE + p * 1024);
                gld16(xr[s][0], xf + st * 2048, loff);
                gld16(xr[s][1], xf + st * 2048 + 1024, loff);
            };
            ldsptr wrd = (ldsptr)smem + (8 * h + q) * 192 + blk * (6144 + 32) + p4 * 8;
            f32x16 acc[6] = {};
            issue(0, 0); issue(1, 1); issue(2, 2);
            auto stage = [&](int s) {
#pragma unroll
                for (int ks = 0; ks < 2; ++ks)
#pragma unroll
                    for (int u = 0; u < 6; ++u) {
                        bf16x8 w = tr_frag(wrd + s * F1_STAGE + ks * 16 * 192 + u * 32, wrd + s * F1_STAGE + ks * 16 * 192 + 4 * 192 + u * 32);
                        acc[u] = MFMA(w, xr[s][ks], acc[u]);
                    }
            };
#pragma unroll 1
            for (int st4 = 0; st4 < 12; st4 += 4) {
#pragma unroll
                for (int s = 0; s < 4; ++s) {
                    wait_vm2<10>(xr[s][0], xr[s][1]);
                    bar();
                    issue(st4 + s + 3, (s + 3) & 3);
                    stage(s);
                }
            }
            wait_vm2<10>(xr[0][0], xr[0][1]); bar(); issue(15, 3); stage(0);
            wait_vm2<10>(xr[1][0], xr[1][1]); bar(); stage(1);
            wait_vm2<5>(xr[2][0], xr[2][1]); bar(); stage(2);
            wait_vm2<0>(xr[3][0], xr[3][1]); bar(); stage(3);
            bar();
            STAMP(1, 1);
#pragma unroll
            for (int j = 0; j < F2_NS; ++j) issue_w(wd, k0 + j, j);
            char* hp = ws + WS_H + ((size_t)T * NKF + u0) * 1024;
#pragma unroll
            for (int u = 0; u < 6; ++u) {
                f32x16 t;
#pragma unroll
                for (int j = 0; j < 8; ++j) {
                    const float g = acc[u][j];
                    t[j] = g * acc[u][8 + j] * __builtin_amdgcn_rcpf(1.0f + EXP2(-LOG2E * g));
                }
                gst16_sc1(hp + u * 1024, loff, pack8(t, 0));
            }
            wait_vm<0>();
            bar();
            if (tid == 0) __hip_atomic_store((gu32*)(ws + WS_FLAG + rb * 128) + gi, 1u, __ATOMIC_RELAXED, __HIP_MEMORY_SCOPE_AGENT);
            STAMP(1, 2);
        } else {
#pragma unroll
            for (int j = 0; j < F2_NS; ++j) issue_w(wd, k0 + j, j);
        }
    }
    const int row0 = 4 * h + q, sw = (row0 >> 1) & 1;
    ldsptr wrd0 = (ldsptr)smem + wid * (F2_NS * 2048) + row0 * 128 + sw * 64 + (p4 & 1) * 32 + blk * 16 + (p4 >> 1) * 8;
    ldsptr wrd1 = (ldsptr)smem + wid * (F2_NS * 2048) + row0 * 128 + (sw ^ 1) * 64 + (p4 & 1) * 32 + blk * 16 + (p4 >> 1) * 8;
    const float g0 = ((const float*)(ws + WS_G))[mb * 64 + (lane & 31)], g1 = ((const float*)(ws + WS_G))[mb * 64 + 32 + (lane & 31)];
    {
        const int glo = k0 / 6, ghi = klast >= 84 ? 14 : klast / 6;
        wait_flags(ws + WS_FLAG + (mb >> 1) * 128, lane, (lane & 31) >= glo && (lane & 31) <= ghi);
    }
    STAMP(1, 3);
    const char* hf0 = ws + WS_H + (size_t)(2 * mb) * NKF * 1024;
    const char* hf1 = hf0 + NKF * 1024;
    f32x16 acc[4] = {};
    bf16x8 ha[8], hb[8];
#pragma unroll
    for (int j = 0; j < F2_NS; ++j) { gld16_sc1(ha[j], hf0 + (k0 + j) * 1024, loff); gld16_sc1(hb[j], hf1 + (k0 + j) * 1024, loff); }
#pragma unroll
    for (int j = 0; j < 22; ++j) {
        const int s = j % F2_NS;
        if (j == 0) wait_vm2<10>(ha[s], hb[s]); else if (j == 1) wait_vm2<12>(ha[s], hb[s]); else if (j == 2) wait_vm2<14>(ha[s], hb[s]);
        else if (j == 3) wait_vm2<16>(ha[s], hb[s]); else if (j == 4) wait_vm2<18>(ha[s], hb[s]); else if (j <= 16) wait_vm2<20>(ha[s], hb[s]);
        else if (j == 17) wait_vm2<18>(ha[s], hb[s]); else if (j == 18) wait_vm2<16>(ha[s], hb[s]); else if (j == 19) wait_vm2<14>(ha[s], hb[s]);
        else if (j == 20) wait_vm2<12>(ha[s], hb[s]); else wait_vm2<10>(ha[s], hb[s]);
        if (j < 21 || wid < 2) {
            bf16x8 w0 = tr_frag(wrd0 + s * 2048, wrd0 + s * 2048 + 1024);
            bf16x8 w1 = tr_frag(wrd1 + s * 2048, wrd1 + s * 2048 + 1024);
            acc[0] = MFMA(w0, ha[s], acc[0]);
            acc[2] = MFMA(w0, hb[s], acc[2]);
            acc[1] = MFMA(w1, ha[s], acc[1]);
            acc[3] = MFMA(w1, hb[s], acc[3]);
        }
        asm volatile("s_waitcnt lgkmcnt(0)" ::: "memory");
        if (j + F2_NS < 22) {
            const int k = min(k0 + j + F2_NS, klast);
            issue_w(wd, k, s);
            gld16_sc1(ha[s], hf0 + k * 1024, loff);
            gld16_sc1(hb[s], hf1 + k * 1024, loff);
        } else {
            issue_w(wo, wid * 8 + (j + F2_NS - 22), s);
        }
    }
    STAMP(1, 4);
    wait_vm<8>();
    bf16x8 wa[2][2];
#pragma unroll
    for (int j = 0; j < 2; ++j) {
        wa[j][0] = tr_frag(wrd0 + (4 + j) * 2048, wrd0 + (4 + j) * 2048 + 1024);
        wa[j][1] = tr_frag(wrd1 + (4 + j) * 2048, wrd1 + (4 + j) * 2048 + 1024);
    }
    asm volatile("s_waitcnt lgkmcnt(0)" : "+v"(wa[0][0]), "+v"(wa[0][1]), "+v"(wa[1][0]), "+v"(wa[1][1]) :: "memory");
    issue_w(wo, wid * 8 + 6, 4);
    issue_w(wo, wid * 8 + 7, 5);
#pragma unroll
    for (int i = 0; i < 16; ++i) { acc[0][i] *= 1.0f - g0; acc[1][i] *= 1.0f - g0; acc[2][i] *= 1.0f - g1; acc[3][i] *= 1.0f - g1; }
    wait_flags(ws + WS_FLAG + 2048 + (mb >> 1) * 128, lane, ((lane & 31) >> 3) == wid);
    STAMP(1, 5);
    const char* of0 = ws + WS_O + ((size_t)(2 * mb) * NH + wid * 8) * 1024;
#pragma unroll
    for (int j = 0; j < 8; ++j) { gld16_sc1(ha[j], of0 + j * 1024, loff); gld16_sc1(hb[j], of0 + NH * 1024 + j * 1024, loff); }
#pragma unroll
    for (int j = 0; j < 8; ++j) {
        bf16x8 w0, w1;
        if (j < 2) { w0 = wa[j][0]; w1 = wa[j][1]; }
        else {
            const int s = (j + 4) % F2_NS;
            w0 = tr_frag(wrd0 + s * 2048, wrd0 + s * 2048 + 1024);
            w1 = tr_frag(wrd1 + s * 2048, wrd1 + s * 2048 + 1024);
        }
        if (j == 0) wait_vm2<14>(ha[j], hb[j]); else if (j == 1) wait_vm2<12>(ha[j], hb[j]); else if (j == 2) wait_vm2<10>(ha[j], hb[j]);
        else if (j == 3) wait_vm2<8>(ha[j], hb[j]); else if (j == 4) wait_vm2<6>(ha[j], hb[j]); else if (j == 5) wait_vm2<4>(ha[j], hb[j]);
        else if (j == 6) wait_vm2<2>(ha[j], hb[j]); else wait_vm2<0>(ha[j], hb[j]);
        acc[0] = MFMA(w0, ha[j], acc[0]);
        acc[2] = MFMA(w0, hb[j], acc[2]);
        acc[1] = MFMA(w1, ha[j], acc[1]);
        acc[3] = MFMA(w1, hb[j], acc[3]);
    }
    STAMP(1, 6);
    bar();
    LDSP f32x4* red = (LDSP f32x4*)smem;
#pragma unroll
    for (int t = 0; t < 4; ++t) {
        if (t != wid) {
            const int slot = (wid - t - 1) & 3;
#pragma unroll
            for (int i = 0; i < 4; ++i) {
                f32x4 v = {acc[t][4 * i], acc[t][4 * i + 1], acc[t][4 * i + 2], acc[t][4 * i + 3]};
                red[((t * 3 + slot) * 4 + i) * 64 + lane] = v;
            }
        }
    }
    bar();
    f32x16 o = (wid == 0) ? acc[0] : (wid == 1) ? acc[1] : (wid == 2) ? acc[2] : acc[3];
#pragma unroll
    for (int s = 0; s < 3; ++s)
#pragma unroll
        for (int i = 0; i < 4; ++i) {
            f32x4 v = red[((wid * 3 + s) * 4 + i) * 64 + lane];
            o[4 * i] += v[0]; o[4 * i + 1] += v[1]; o[4 * i + 2] += v[2]; o[4 * i + 3] += v[3];
        }
    char* op = (char*)out + (size_t)(mb * 64 + (wid >> 1) * 32 + (lane & 31)) * 1024 + (nb * 64 + (wid & 1) * 32 + h * 16) * 2;
    *(bf16x8*)op = pack8(o, 0);
    *(bf16x8*)(op + 16) = pack8(o, 8);
    STAMPE(1, 7);
}

constexpr int TPC = 8, NCH = NTT / TPC, A_NS = 3;
constexpr int A_SLOT = TPC * 1024 + TPC * 1040;
constexpr int K2_LDS = A_SLOT * A_NS;
static_assert(K2_LDS >= F1_STAGE * F1_NS && K2_LDS >= 4 * F2_NS * 2048, "lds");

__device__ __forceinline__ f32x16 attn_fallback(const char* kf, const char* vf, bf16x8 qf, int lane) {
    f32x16 o = {};
    float m = -INFINITY;
    const bool isv = ((lane >> 4) & 1) == 0;
    const u32x4 ones = {0x3f803f80u, 0x3f803f80u, 0x3f803f80u, 0x3f803f80u};
#pragma unroll 1
    for (int t = 0; t < NTT; ++t) {
        bf16x8 kk = *(const bf16x8*)(kf + t * 1024 + lane * 16);
        const char* vp = vf + t * 1024 + (lane >> 5) * 256 + (lane & 15) * 16;
        u32x4 v0 = *(const u32x4*)vp, v1 = *(const u32x4*)(vp + 512);
        if (!isv) { v0 = ones; v1 = ones; }
        f32x16 z = {};
        f32x16 s = MFMA(kk, qf, z);
        float tm = s[0];
#pragma unroll
        for (int i = 1; i < 16; ++i) tm = fmaxf(tm, s[i]);
        tm = fmaxf(tm, __shfl_xor(tm, 32));
        const float mn = fmaxf(m, tm);
        const float al = EXP2(m - mn);
        m = mn;
#pragma unroll
        for (int i = 0; i < 16; ++i) { o[i] *= al; s[i] = EXP2(s[i] - mn); }
        o = MFMA(__builtin_bit_cast(bf16x8, v0), pack8(s, 0), o);
        o = MFMA(__builtin_bit_cast(bf16x8, v1), pack8(s, 8), o);
    }
    return o;
}

__device__ __forceinline__ void attn_role(int b, char* smem, char* __restrict__ ws) {
    const int tid = threadIdx.x, lane = tid & 63;
    const int wid = __builtin_amdgcn_readfirstlane(tid >> 6);
    const int xcd = b & 7, idx = b >> 3;
    const int head = xcd * 4 + (idx & 3), qb = idx >> 2;
    STAMP0(2);
    __builtin_amdgcn_s_setprio(AP0);
    const int T = qb * 4 + wid;
    const unsigned lds0 = (unsigned)(uintptr_t)smem;
    const char* kf = ws + WS_K + (size_t)head * NTT * 1024;
    const char* vf = ws + WS_V + (size_t)head * NTT * 1024;
    const unsigned loff = lane * 16;
    const bool isv = ((lane >> 4) & 1) == 0;
    const unsigned voff = (lane >> 5) * 256 + (lane & 15) * 16;
    const bf16x8 qf = *(const bf16x8*)(ws + WS_Q + ((size_t)head * NTT + T) * 1024 + loff);
    const bf16x8 k0 = *(const bf16x8*)(kf + loff);
    bf16x8 kk = *(const bf16x8*)(kf + 1024 + loff);
    u32x4 g0 = *(const u32x4*)(vf + voff), g1 = *(const u32x4*)(vf + 512 + voff);
    const float gate = ((const float*)(ws + WS_G))[T * 32 + (lane & 31)];
    const char* ksrc = kf + (2 + 2 * wid) * 1024;
    const char* vsrc = vf + (1 + 2 * wid) * 1024;
    const unsigned kdst = lds0 + wid * 2048, vdst = lds0 + TPC * 1024 + wid * 2080;
    auto issue = [&](int c, int slot) {
        glds16(ksrc + c * (TPC * 1024), loff, kdst + slot * A_SLOT);
        glds16(vsrc + c * (TPC * 1024), loff, vdst + slot * A_SLOT);
        glds16(ksrc + c * (TPC * 1024) + 1024, loff, kdst + slot * A_SLOT + 1024);
        glds16(vsrc + c * (TPC * 1024) + 1024, loff, vdst + slot * A_SLOT + 1040);
    };
    issue(0, 0);
    const u32x4 ones = {0x3f803f80u, 0x3f803f80u, 0x3f803f80u, 0x3f803f80u};
    if (tid < A_NS * TPC) *(LDSP u32x4*)((ldsptr)smem + (tid / TPC) * A_SLOT + TPC * 1024 + (tid % TPC) * 1040 + 1024) = ones;
    ldsptr krd = (ldsptr)smem + lane * 16;
    ldsptr vrd0 = (ldsptr)smem + TPC * 1024 + (isv ? voff : 1024);
    ldsptr vrd1 = (ldsptr)smem + TPC * 1024 + (isv ? 512 + voff : 1024);
    f32x16 z = {};
    f32x16 s = MFMA(k0, qf, z);
    issue(1, 1);
    float m = s[0];
#pragma unroll
    for (int i = 1; i < 16; ++i) m = fmaxf(m, s[i]);
    m = fmaxf(m, __shfl_xor(m, 32));
    f32x16 negm;
#pragma unroll
    for (int i = 0; i < 16; ++i) { negm[i] = -m; s[i] -= m; }
    bf16x8 v0 = __builtin_bit_cast(bf16x8, isv ? g0 : ones), v1 = __builtin_bit_cast(bf16x8, isv ? g1 : ones);
    f32x16 o = {};
    u32x4 p1 = {0, 0, 0, 0};
    bf16x8 v1p = v1;
    int slot = 0, wslot = A_NS - 1;
    STAMP(2, 1);
#pragma unroll 1
    for (int c = 0; c < NCH; ++c) {
        if (c + 1 < NCH) wait_vm<4>(); else wait_vm<0>();
        bar();
        if (c + 2 < NCH) issue(c + 2, wslot);
        if (c == 4) __builtin_amdgcn_s_setprio(AP1);
        if (c == 6) __builtin_amdgcn_s_setprio(AP2);
        const int so = slot * A_SLOT;
#pragma unroll
        for (int i = 0; i < TPC; ++i) {
            SBAR();
            f32x16 sn = MFMA(kk, qf, negm);
            SBAR();
            kk = lds_b128(krd + so + i * 1024);
            bf16x8 nv0 = lds_b128(vrd0 + so + i * 1040);
            SBAR();
            u32x4 p0;
            { float a = EXP2(s[0]), b2 = EXP2(s[1]), c2 = EXP2(s[2]), d = EXP2(s[3]); p0[0] = cvtpk(a, b2); p0[1] = cvtpk(c2, d); }
            SBAR();
            o = MFMA(v1p, __builtin_bit_cast(bf16x8, p1), o);
            SBAR();
            v1p = v1;
            v1 = lds_b128(vrd1 + so + i * 1040);
            SBAR();
            { float a = EXP2(s[4]), b2 = EXP2(s[5]), c2 = EXP2(s[6]), d = EXP2(s[7]); p0[2] = cvtpk(a, b2); p0[3] = cvtpk(c2, d); }
            SBAR();
            float e8 = EXP2(s[8]), e9 = EXP2(s[9]);
            SBAR();
            o = MFMA(v0, __builtin_bit_cast(bf16x8, p0), o);
            SBAR();
            v0 = nv0;
            { float c2 = EXP2(s[10]), d = EXP2(s[11]); p1[0] = cvtpk(e8, e9); p1[1] = cvtpk(c2, d); }
            SBAR();
            { float a = EXP2(s[12]), b2 = EXP2(s[13]), c2 = EXP2(s[14]), d = EXP2(s[15]); p1[2] = cvtpk(a, b2); p1[3] = cvtpk(c2, d); }
            SBAR();
            s = sn;
        }
        wslot = slot;
        slot = (slot == A_NS - 1) ? 0 : slot + 1;
    }
    o = MFMA(v1p, __builtin_bit_cast(bf16x8, p1), o);
    STAMP(2, 2);
    float chk = 0.f;
#pragma unroll
    for (int i = 0; i < 8; ++i) chk += fabsf(o[i]) * 0.125f;
    const bool bad = !(o[8] <= 1.8446744e19f) || !(chk <= 3.0e38f);
    if (__builtin_expect(__any(bad), 0)) o = attn_fallback(kf, vf, qf, lane);
    const float rl = gate * __builtin_amdgcn_rcpf(o[8]);
    gst16_sc1(ws + WS_O + ((size_t)T * NH + head) * 1024, loff, pack8(o, 0, rl));
    wait_vm<0>();
    bar();
    if (tid == 0) __hip_atomic_store((gu32*)(ws + WS_FLAG + 2048 + qb * 128) + head, 1u, __ATOMIC_RELAXED, __HIP_MEMORY_SCOPE_AGENT);
    STAMPE(2, 3);
}

__global__ __launch_bounds__(256, 3) void k2_main(const u16* __restrict__ wg, const u16* __restrict__ wu, const u16* __restrict__ wd,
                                                  const u16* __restrict__ wo, char* __restrict__ ws, u16* __restrict__ out) {
    extern __shared__ __attribute__((aligned(16))) char smem[];
    if (blockIdx.x < 256) ffn_role(blockIdx.x, smem, wg, wu, wd, wo, ws, out);
    else attn_role(blockIdx.x - 256, smem, ws);
}

extern "C" void kernel_launch(void* const* d_in, const int* in_sizes, int n_in, void* d_out, int out_size, void* d_ws,
                              size_t ws_size, hipStream_t stream) {
    const u16* x = (const u16*)d_in[0];
    const u16* wqkv = (const u16*)d_in[1];
    const u16* wo = (const u16*)d_in[2];
    const u16* wg = (const u16*)d_in[4];
    const u16* wu = (const u16*)d_in[5];
    const u16* wd = (const u16*)d_in[6];
    const u16* gw = (const u16*)d_in[7];
    char* ws = (char*)d_ws;
    (void)hipFuncSetAttribute((const void*)k1_qkv, hipFuncAttributeMaxDynamicSharedMemorySize, K1_LDS);
    k1_qkv<<<256, 512, K1_LDS, stream>>>(x, wqkv, gw, ws);
    k2_main<<<768, 256, K2_LDS, stream>>>(wg, wu, wd, wo, ws, (u16*)d_out);
}
```

```cpp
#include <hip/hip_runtime.h>
#include <hip/hip_bf16.h>
#include <stdint.h>

typedef unsigned short u16;
typedef unsigned long long u64;
typedef __attribute__((ext_vector_type(8))) __bf16 bf16x8;
typedef __attribute__((ext_vector_type(2))) __bf16 bf16x2;
typedef __attribute__((ext_vector_type(4))) short s16x4;
typedef __attribute__((ext_vector_type(16))) float f32x16;
typedef __attribute__((ext_vector_type(4))) float f32x4;
typedef __attribute__((ext_vector_type(2))) float f32x2;
typedef __attribute__((ext_vector_type(4))) unsigned u32x4;
#define LDSP __attribute__((address_space(3)))
typedef LDSP char* ldsptr;
typedef __attribute__((address_space(1))) unsigned gu32;
typedef __attribute__((address_space(1))) u64 gu64;

constexpr int S_ = 2048, F_ = 1376, NH = 32;
constexpr int NTT = S_ / 32;
constexpr int NKF = F_ / 16;
constexpr float LOG2E = 1.4426950408889634f;
constexpr float QSCALE = 0.25f * LOG2E;

constexpr size_t WS_EP = 0;
constexpr size_t WS_FQ = 1024;
constexpr size_t WS_FH = WS_FQ + 2048;
constexpr size_t WS_FO = WS_FH + 2048;
constexpr size_t WS_G = WS_FO + 16384;
constexpr size_t WS_Q = 262144;
constexpr size_t WS_K = WS_Q + (size_t)NH * NTT * 1024;
constexpr size_t WS_V = WS_K + (size_t)NH * NTT * 1024;
constexpr size_t WS_O = WS_V + (size_t)NH * NTT * 1024;
constexpr size_t WS_H = WS_O + (size_t)NTT * 32 * 1024;

constexpr int LDS_BYTES = 163840;
constexpr int SLOT = 28672;
__host__ __device__ constexpr int phys(int st) { return 20480 + ((st + 4) % 5) * SLOT; }
constexpr int F1OFF = 4064, F1UP = 12320;
constexpr int CHK = 16512, KOFF = 8320;
constexpr int WO_LO = 130048, POLL = 162816, XCH = 49536, RING = 6144;

#define MFMA(a, b, c) __builtin_amdgcn_mfma_f32_32x32x16_bf16((a), (b), (c), 0, 0, 0)
#define SBAR() __builtin_amdgcn_sched_barrier(0)
#define EXP2(x) __builtin_amdgcn_exp2f(x)
#define RLX_AGENT __ATOMIC_RELAXED, __HIP_MEMORY_SCOPE_AGENT
#define STAMP(i)
#define STAMPC(i)

__device__ __forceinline__ unsigned cvtpk(float lo, float hi) {
    f32x2 v = {lo, hi};
    return __builtin_bit_cast(unsigned, __builtin_convertvector(v, bf16x2));
}
__device__ __forceinline__ void glds16(const void* sbase, unsigned voff, unsigned lds_base) {
    asm volatile("s_mov_b32 m0, %2\n\ts_nop 0\n\tglobal_load_lds_dwordx4 %0, %1" :: "v"(voff), "s"(sbase), "s"(lds_base) : "memory");
}
__device__ __forceinline__ void glds16_sc1(const void* sbase, unsigned voff, unsigned lds_base) {
    asm volatile("s_mov_b32 m0, %2\n\ts_nop 0\n\tglobal_load_lds_dwordx4 %0, %1 sc1" :: "v"(voff), "s"(sbase), "s"(lds_base) : "memory");
}
__device__ __forceinline__ void glds4_sc1(const void* sbase, unsigned voff, unsigned lds_base) {
    asm volatile("s_mov_b32 m0, %2\n\ts_nop 0\n\tglobal_load_lds_dword %0, %1 sc1" :: "v"(voff), "s"(sbase), "s"(lds_base) : "memory");
}
__device__ __forceinline__ void gld16_sc1(bf16x8& r, const void* sbase, unsigned voff) {
    asm volatile("global_load_dwordx4 %0, %1, %2 sc1" : "=v"(r) : "v"(voff), "s"(sbase) : "memory");
}
__device__ __forceinline__ void gld16_sc1_tied(bf16x8& r, const void* sbase, unsigned voff) {
    asm volatile("global_load_dwordx4 %0, %1, %2 sc1" : "+v"(r) : "v"(voff), "s"(sbase) : "memory");
}
__device__ __forceinline__ void gld4_sc1(float& r, const void* sbase, unsigned voff) {
    asm volatile("global_load_dword %0, %1, %2 sc1" : "=v"(r) : "v"(voff), "s"(sbase) : "memory");
}
__device__ __forceinline__ void gst16_sc1(void* sbase, unsigned voff, bf16x8 v) {
    asm volatile("global_store_dwordx4 %0, %1, %2 sc1\n\ts_nop 1" :: "v"(voff), "v"(v), "s"(sbase) : "memory");
}
__device__ __forceinline__ void gst4_sc1(void* sbase, unsigned voff, float v) {
    asm volatile("global_store_dword %0, %1, %2 sc1" :: "v"(voff), "v"(v), "s"(sbase) : "memory");
}
template <int N> __device__ __forceinline__ void wait_vm() { asm volatile("s_waitcnt vmcnt(%0)" ::"i"(N) : "memory"); }
template <int N> __device__ __forceinline__ void wait_vm1(bf16x8& a) { asm volatile("s_waitcnt vmcnt(%1)" : "+v"(a) : "i"(N) : "memory"); }
template <int N> __device__ __forceinline__ void wait_vm2(bf16x8& a, bf16x8& b) {
    asm volatile("s_waitcnt vmcnt(%2)" : "+v"(a), "+v"(b) : "i"(N) : "memory");
}
__device__ __forceinline__ void bar() { asm volatile("s_waitcnt lgkmcnt(0)\n\ts_barrier" ::: "memory"); }
__device__ __forceinline__ bf16x8 lds_b128(ldsptr p) { return *(LDSP bf16x8*)p; }
__device__ __forceinline__ bf16x8 tr_frag(ldsptr p0, ldsptr p1) {
    s16x4 a = __builtin_amdgcn_ds_read_tr16_b64_v4i16((LDSP s16x4*)p0), b = __builtin_amdgcn_ds_read_tr16_b64_v4i16((LDSP s16x4*)p1);
    typedef __attribute__((ext_vector_type(8))) short s16x8;
    s16x8 v = {a[0], a[1], a[2], a[3], b[0], b[1], b[2], b[3]};
    return __builtin_bit_cast(bf16x8, v);
}
__device__ __forceinline__ bf16x8 pack8(const f32x16& a, int o, float sc) {
    u32x4 v = {cvtpk(a[o] * sc, a[o + 1] * sc), cvtpk(a[o + 2] * sc, a[o + 3] * sc),
               cvtpk(a[o + 4] * sc, a[o + 5] * sc), cvtpk(a[o + 6] * sc, a[o + 7] * sc)};
    return __builtin_bit_cast(bf16x8, v);
}
__device__ __forceinline__ bf16x8 pack8(const f32x16& a, int o) {
    u32x4 v = {cvtpk(a[o], a[o + 1]), cvtpk(a[o + 2], a[o + 3]), cvtpk(a[o + 4], a[o + 5]), cvtpk(a[o + 6], a[o + 7])};
    return __builtin_bit_cast(bf16x8, v);
}
__device__ __forceinline__ void spin_flags(const char* p, bool need, u64 tag) {
#pragma unroll 1
    for (int spin = 0; spin < (1 << 20); ++spin) {
        const u64 v = __hip_atomic_load((gu64*)p, RLX_AGENT);
        if (__all(!need || v == tag)) break;
        __builtin_amdgcn_s_sleep(2);
    }
}

__host__ __device__ constexpr int grp(int s, int nq) { return s <= 7 ? nq : (s <= 15 ? 3 : 0); }
__host__ __device__ constexpr int younger(int s, int nq, int ns) {
    int n = 0;
    for (int t = s + 1; t <= (s == 0 ? 4 : s + 3); ++t) n += grp(t, nq);
    if (s >= 8 && s <= 11) n += ns;
    return n;
}

__device__ __forceinline__ f32x16 attn_fallback(const char* kf, const char* vf, bf16x8 qf, int lane) {
    f32x16 o = {};
    float m = -INFINITY;
    const bool isv = ((lane >> 4) & 1) == 0;
    const u32x4 ones = {0x3f803f80u, 0x3f803f80u, 0x3f803f80u, 0x3f803f80u};
    const unsigned voff = (lane >> 5) * 256 + (lane & 15) * 16;
#pragma unroll 1
    for (int t = 0; t < NTT; ++t) {
        bf16x8 kk, a0, a1;
        gld16_sc1(kk, kf + t * 1024, lane * 16);
        gld16_sc1(a0, vf + t * 1024, voff);
        gld16_sc1(a1, vf + t * 1024 + 512, voff);
        wait_vm1<0>(kk);
        wait_vm2<0>(a0, a1);
        u32x4 v0 = __builtin_bit_cast(u32x4, a0), v1 = __builtin_bit_cast(u32x4, a1);
        if (!isv) { v0 = ones; v1 = ones; }
        f32x16 z = {};
        f32x16 s = MFMA(kk, qf, z);
        float tm = s[0];
#pragma unroll
        for (int i = 1; i < 16; ++i) tm = fmaxf(tm, s[i]);
        tm = fmaxf(tm, __shfl_xor(tm, 32));
        const float mn = fmaxf(m, tm);
        const float al = EXP2(m - mn);
        m = mn;
#pragma unroll
        for (int i = 0; i < 16; ++i) { o[i] *= al; s[i] = EXP2(s[i] - mn); }
        o = MFMA(__builtin_bit_cast(bf16x8, v0), pack8(s, 0), o);
        o = MFMA(__builtin_bit_cast(bf16x8, v1), pack8(s, 8), o);
    }
    return o;
}

__global__ __launch_bounds__(512, 2) void fused(const u16* __restrict__ x, const u16* __restrict__ wqkv, const u16* __restrict__ wo,
                                                const u16* __restrict__ wg, const u16* __restrict__ wu, const u16* __restrict__ wd,
                                                const u16* __restrict__ gatew, char* __restrict__ ws, u16* __restrict__ out) {
    extern __shared__ __attribute__((aligned(16))) char smem[];
    STAMP(0); STAMPC(61);
    const int tid = threadIdx.x, lane = tid & 63;
    const int wid = __builtin_amdgcn_readfirstlane(tid >> 6);
    const int xcd = blockIdx.x & 7, idx = blockIdx.x >> 3;
    const unsigned lds0 = (unsigned)(uintptr_t)smem;
    const unsigned loff = lane * 16;
    const int r = lane & 31, h = lane >> 5, q = (lane & 15) >> 2, p4 = lane & 3, blk = (lane >> 4) & 1;
    const int rb = (xcd & 1) * 8 + (idx & 7), cg = (xcd >> 1) * 4 + (idx >> 3);
    const int tt = wid & 3, ch = wid >> 2, hg = cg >> 1, odd = cg & 1;
    unsigned xoff[2], woff[2], foff[3];
#pragma unroll
    for (int e = 0; e < 2; ++e) {
        const int row = wid * 16 + e * 8 + (lane >> 3);
        xoff[e] = (unsigned)((rb * 128 + row) * 1024 + (((lane & 7) ^ ((row >> 1) & 7)) * 16));
    }
    const int wp0 = ch ? 2 * wid - 4 : wid;
#pragma unroll
    for (int e = 0; e < 2; ++e) {
        const int n = (wp0 + e) * 64 + lane, kr = n / 12, c = n % 12;
        woff[e] = (unsigned)(kr * 3072 + hg * 128 + (c < 8 ? odd * 2048 + c * 16 : 1024 - 128 + odd * 64 + c * 16));
    }
    if (wid == 7) glds16(gatew, loff, lds0);
    auto issue_q = [&](int st, bool two) {
        glds16((const char*)x + st * 128, xoff[0], lds0 + phys(st) + wid * 2048);
        glds16((const char*)wqkv + st * (64 * 3072), woff[0], lds0 + phys(st) + 16384 + wp0 * 1024);
        glds16((const char*)x + st * 128, xoff[1], lds0 + phys(st) + wid * 2048 + 1024);
        if (two) glds16((const char*)wqkv + st * (64 * 3072), woff[1], lds0 + phys(st) + 16384 + wp0 * 1024 + 1024);
    };
#pragma unroll
    for (int st = 0; st < 5; ++st) issue_q(st, ch != 0);
    STAMP(1);
    const u64 ep = *(const __attribute__((address_space(4))) u64*)(uintptr_t)(ws + WS_EP);
    const u64 tag = (ep ^ (ep >> 31)) * 0x9E3779B97F4A7C15ull + 0xD1B54A32D192ED03ull;
    const int u0 = cg < 14 ? cg * 6 : 80;
#pragma unroll
    for (int e = 0; e < 3; ++e) {
        const int n = (tt * 3 + e) * 64 + lane, kr = n / 12, c = n % 12;
        foff[e] = (unsigned)(kr * (F_ * 2) + u0 * 32 + c * 16);
    }
    const char* fsrc = (const char*)(ch ? wu : wg);
    const unsigned fdst = lds0 + F1OFF + ch * F1UP + tt * 3072;
    auto issue_f = [&](int st) {
#pragma unroll
        for (int e = 0; e < 3; ++e) glds16(fsrc + (st - 8) * (64 * F_ * 2), foff[e], fdst + phys(st) + e * 1024);
    };
    bf16x8 xr[32];
    float gate;
    const int T = rb * 4 + tt;
    {
        ldsptr xrd[4];
#pragma unroll
        for (int ks = 0; ks < 4; ++ks) xrd[ks] = (ldsptr)smem + (tt * 32 + r) * 128 + (((2 * ks + h) ^ ((r >> 1) & 7)) * 16);
        ldsptr wrd = (ldsptr)smem + 16384 + (8 * h + q) * 192 + blk * 32 + p4 * 8 + ch * 128;
        ldsptr grd = (ldsptr)smem + h * 16;
        float gacc = 0.f;
        auto qkv = [&](auto var) {
            constexpr int VAR = decltype(var)::value;
            constexpr int NQ = VAR == 2 ? 4 : 3, NS = VAR == 2 ? 3 : 4;
            f32x16 a0 = {}, a1 = {};
            bf16x8 w0, w1, gw;
            auto rd = [&](int g, bf16x8& b0, bf16x8& b1, bf16x8& bg) {
                const int so = phys(g >> 2), ks = g & 3;
                xr[g] = lds_b128(xrd[ks] + so);
                b0 = tr_frag(wrd + so + ks * 3072, wrd + so + ks * 3072 + 768);
                if (VAR != 2) b1 = tr_frag(wrd + so + ks * 3072 + 64, wrd + so + ks * 3072 + 64 + 768);
                bg = lds_b128(grd + g * 32);
            };
            wait_vm<younger(0, NQ, NS)>();
            bar();
            STAMP(2);
            rd(0, w0, w1, gw);
#pragma unroll
            for (int g = 0; g < 32; ++g) {
                if ((g & 3) == 3 && g < 31) {
                    const int s = (g >> 2) + 1;
                    if (s == 1) wait_vm<younger(1, NQ, NS)>(); else if (s == 2) wait_vm<younger(2, NQ, NS)>();
                    else if (s == 3) wait_vm<younger(3, NQ, NS)>(); else if (s == 4) wait_vm<younger(4, NQ, NS)>();
                    else if (s == 5) wait_vm<younger(5, NQ, NS)>(); else if (s == 6) wait_vm<younger(6, NQ, NS)>();
                    else wait_vm<younger(7, NQ, NS)>();
                    bar();
                    STAMP(2 + s);
                    if (s + 4 <= 7) issue_q(s + 4, VAR == 2); else issue_f(s + 4);
                }
                bf16x8 n0 = w0, n1 = w1, ng = gw;
                if (g < 31) rd(g + 1, n0, n1, ng);
                SBAR();
                if (VAR == 1) { a0 = MFMA(xr[g], w0, a0); a1 = MFMA(xr[g], w1, a1); }
                else { a0 = MFMA(w0, xr[g], a0); if (VAR == 0) a1 = MFMA(w1, xr[g], a1); }
#pragma unroll
                for (int j = 0; j < 4; ++j) {
                    const bf16x2 xa = {xr[g][2 * j], xr[g][2 * j + 1]}, ga = {gw[2 * j], gw[2 * j + 1]};
                    gacc = __builtin_amdgcn_fdot2_f32_bf16(xa, ga, gacc, false);
                }
                SBAR();
                w0 = n0; w1 = n1; gw = ng;
            }
            STAMP(10);
            gacc += __shfl_xor(gacc, 32);
            gate = __builtin_amdgcn_rcpf(1.0f + EXP2(-LOG2E * gacc));
            if (VAR == 0) {
                char* qp = ws + WS_Q + ((size_t)(4 * hg) * NTT + T) * 1024;
                gst16_sc1(qp, loff, pack8(a0, 0, QSCALE));
                gst16_sc1(qp + NTT * 1024, loff, pack8(a0, 8, QSCALE));
                gst16_sc1(qp + 2 * NTT * 1024, loff, pack8(a1, 0, QSCALE));
                gst16_sc1(qp + 3 * NTT * 1024, loff, pack8(a1, 8, QSCALE));
            } else if (VAR == 1) {
                char* vp = ws + WS_V + ((size_t)(4 * hg) * NTT + T) * 1024;
                const unsigned vo = blk * (NTT * 1024) + h * 256 + (lane & 15) * 16;
                gst16_sc1(vp, vo, pack8(a0, 0));
                gst16_sc1(vp + 512, vo, pack8(a0, 8));
                gst16_sc1(vp + 2 * NTT * 1024, vo, pack8(a1, 0));
                gst16_sc1(vp + 2 * NTT * 1024 + 512, vo, pack8(a1, 8));
            } else {
                char* kp = ws + WS_K + ((size_t)(4 * hg + 2 * odd) * NTT + T) * 1024;
                gst16_sc1(kp, loff, pack8(a0, 0));
                gst16_sc1(kp + NTT * 1024, loff, pack8(a0, 8));
                gst4_sc1(ws + WS_G + (cg * S_ + T * 32) * 4, r * 4, gate);
            }
        };
        if (ch) qkv(std::integral_constant<int, 2>{});
        else if (odd) qkv(std::integral_constant<int, 1>{});
        else qkv(std::integral_constant<int, 0>{});
    }
    const int head = xcd * 4 + (idx & 3), qb = idx >> 2;
    const int AT = qb * 8 + wid;
    const char* kf = ws + WS_K + (size_t)head * NTT * 1024;
    const char* vf = ws + WS_V + (size_t)head * NTT * 1024;
    const bool isv = blk == 0;
    const unsigned voff = h * 256 + (lane & 15) * 16;
    const u32x4 ones = {0x3f803f80u, 0x3f803f80u, 0x3f803f80u, 0x3f803f80u};
    bf16x8 qf, k0, kk, g0, g1;
    float agate;
    {
        ldsptr wrd = (ldsptr)smem + F1OFF + (8 * h + q) * 192 + blk * F1UP + p4 * 8 + ch * 96;
        f32x16 acc[3] = {};
        bf16x8 w[3];
        u64 pollv = 0;
        const char* fq = ws + WS_FQ + xcd * 256;
        auto rd = [&](int g, bf16x8 (&b)[3]) {
            const int so = phys(8 + (g >> 2)) + (g & 3) * 3072;
#pragma unroll
            for (int u = 0; u < 3; ++u) b[u] = tr_frag(wrd + so + u * 32, wrd + so + u * 32 + 768);
        };
        STAMP(11);
        wait_vm<younger(8, 3, 3)>();
        bar();
        issue_f(12);
        STAMP(12);
        rd(0, w);
#pragma unroll
        for (int g = 0; g < 32; ++g) {
            if ((g & 3) == 3 && g < 31) {
                const int s = 8 + (g >> 2) + 1;
                if (s == 9) wait_vm<younger(9, 3, 3)>();
                else if (s == 10) wait_vm<6>();
                else if (s == 12) wait_vm<9>();
                else if (s == 13) wait_vm<6>();
                else if (s == 14) {
                    asm volatile("s_waitcnt vmcnt(0)" : "+v"(pollv) :: "memory");
                    if (wid == 0 && !__all(pollv == tag)) spin_flags(fq + r * 8, true, tag);
                }
                bar();
                STAMP(4 + s);
                if (s <= 11) issue_f(s + 4);
                if (s == 10 && tid == 0) __hip_atomic_store((gu64*)(ws + WS_FQ + hg * 256) + odd * 16 + rb, tag, RLX_AGENT);
                if (s == 13 && wid == 0) asm volatile("global_load_dwordx2 %0, %1, %2 sc1" : "+v"(pollv) : "v"(r * 8), "s"(fq) : "memory");
                if (s == 14) {
                    gld16_sc1(qf, ws + WS_Q + ((size_t)head * NTT + AT) * 1024, loff);
                    gld16_sc1(k0, kf, loff);
                    gld16_sc1(g0, vf, voff);
                    gld16_sc1(g1, vf + 512, voff);
                    gld16_sc1(kk, kf + 1024, loff);
                    gld4_sc1(agate, ws + WS_G + (2 * xcd * S_ + AT * 32) * 4, r * 4);
#pragma unroll
                    for (int c = 0; c < 6; ++c) {
                        glds16_sc1(vf + (8 * c + 1 + wid) * 1024, loff, lds0 + c * CHK + wid * 1040);
                        glds16_sc1(kf + (8 * c + 2 + wid) * 1024, loff, lds0 + c * CHK + KOFF + wid * 1024);
                    }
                }
                if (s == 15) {
                    glds16_sc1(vf + (49 + wid) * 1024, loff, lds0 + 6 * CHK + wid * 1040);
                    glds16_sc1(kf + (50 + wid) * 1024, loff, lds0 + 6 * CHK + KOFF + wid * 1024);
                    if (wid < 7) glds16_sc1(vf + (57 + wid) * 1024, loff, lds0 + 7 * CHK + wid * 1040);
                    if (wid < 6) glds16_sc1(kf + (58 + wid) * 1024, loff, lds0 + 7 * CHK + KOFF + wid * 1024);
                    if (wid == 7) *(LDSP u32x4*)((ldsptr)smem + (lane >> 3) * CHK + (lane & 7) * 1040 + 1024) = ones;
                }
            }
            bf16x8 n[3] = {w[0], w[1], w[2]};
            if (g < 31) rd(g + 1, n);
            SBAR();
#pragma unroll
            for (int u = 0; u < 3; ++u) acc[u] = MFMA(w[u], xr[g], acc[u]);
            SBAR();
            w[0] = n[0]; w[1] = n[1]; w[2] = n[2];
        }
        STAMP(20);
        char* hp = ws + WS_H + ((size_t)T * NKF + u0 + ch * 3) * 1024;
        const float og = 1.0f - gate;
#pragma unroll
        for (int u = 0; u < 3; ++u) {
            f32x16 t;
#pragma unroll
            for (int j = 0; j < 8; ++j) {
                const float g = acc[u][j];
                t[j] = g * acc[u][8 + j] * og * __builtin_amdgcn_rcpf(1.0f + EXP2(-LOG2E * g));
            }
            gst16_sc1(hp + u * 1024, loff, pack8(t, 0));
        }
    }
    const int mb = xcd * 4 + (idx & 3), nb = idx >> 2;
    const int mh = wid & 1, kq = wid >> 1;
    const int fk0 = wid * 11 - (wid > 6 ? 1 : 0), fnk = wid < 6 ? 11 : 10;
    unsigned doff;
    {
        const int row = lane >> 3;
        doff = (unsigned)(row * 1024 + nb * 128 + (((lane & 7) ^ (((row >> 1) & 1) << 2)) * 16));
    }
    const int row0 = 4 * h + q, sw = (row0 >> 1) & 1;
    const int wfo0 = row0 * 128 + sw * 64 + (p4 & 1) * 32 + blk * 16 + (p4 >> 1) * 8, wfo1 = wfo0 ^ 64;
    const char* hfa = ws + WS_H + (size_t)(2 * mb + mh) * NKF * 1024;
    const char* hfb = ws + WS_H + (size_t)(2 * mb + 1 - mh) * NKF * 1024;
    f32x16 o = {};
    f32x16 acc[4] = {};
    {
        asm volatile("s_waitcnt vmcnt(15)" : "+v"(qf), "+v"(k0), "+v"(kk), "+v"(g0), "+v"(g1), "+v"(agate) :: "memory");
        STAMP(21);
        f32x16 z = {};
        f32x16 s = MFMA(k0, qf, z);
        bar();
#pragma unroll
        for (int e = 0; e < 4; ++e) glds16((const char*)wo + (wid * 4 + e) * 8192, doff, lds0 + WO_LO + (wid * 4 + e) * 1024);
        float m = s[0];
#pragma unroll
        for (int i = 1; i < 16; ++i) m = fmaxf(m, s[i]);
        m = fmaxf(m, __shfl_xor(m, 32));
        f32x16 negm;
#pragma unroll
        for (int i = 0; i < 16; ++i) { negm[i] = -m; s[i] -= m; }
        bf16x8 v0 = isv ? g0 : __builtin_bit_cast(bf16x8, ones), v1 = isv ? g1 : __builtin_bit_cast(bf16x8, ones);
        u32x4 p1 = {0, 0, 0, 0};
        bf16x8 v1p = v1;
        ldsptr krd = (ldsptr)smem + KOFF + lane * 16;
        ldsptr vrd0 = (ldsptr)smem + (isv ? voff : 1024);
        ldsptr vrd1 = (ldsptr)smem + (isv ? 512 + voff : 1024);
        ldsptr wr0 = (ldsptr)smem + wid * RING + wfo0, wr1 = (ldsptr)smem + wid * RING + wfo1;
        bf16x8 ha[3] = {}, hb[3] = {};
        STAMP(22);
#pragma unroll 1
        for (int c = 0; c < 8; ++c) {
            if (c == 1) {
                wait_vm<4>();
                bar();
                if (tid == 0) __hip_atomic_store((gu64*)(ws + WS_FH + rb * 128) + cg, tag, RLX_AGENT);
            } else if (c == 2) {
                if (wid == 0) glds4_sc1(ws + WS_FH + (mb >> 1) * 128, r * 4, lds0 + POLL);
            } else if (c == 3) {
                if (wid == 0) {
                    wait_vm<0>();
                    const u64 v = *(LDSP u64*)((ldsptr)smem + POLL + (lane & 15) * 8);
                    if (!__all((lane & 15) == 15 || v == tag)) spin_flags(ws + WS_FH + (mb >> 1) * 128 + (lane & 15) * 8, (lane & 15) != 15, tag);
                }
                bar();
            }
            STAMP(30 + c);
            const int j0 = 3 * c - 12;
            bf16x8 wf;
#pragma unroll
            for (int i = 0; i < 8; ++i) {
                const int e = i / 3, ph = i % 3;
                const bool fon = j0 + e >= 0 && j0 + e < fnk;
                SBAR();
                f32x16 sn = MFMA(kk, qf, negm);
                SBAR();
                kk = lds_b128(krd + i * 1024);
                bf16x8 nv0 = lds_b128(vrd0 + i * 1040);
                if (ph == 0) wait_vm2<8>(ha[e], hb[e]);
                if (ph < 2) wf = tr_frag((ph ? wr1 : wr0) + e * 2048, (ph ? wr1 : wr0) + e * 2048 + 1024);
                SBAR();
                u32x4 p0;
                { float a = EXP2(s[0]), b2 = EXP2(s[1]), c2 = EXP2(s[2]), d = EXP2(s[3]); p0[0] = cvtpk(a, b2); p0[1] = cvtpk(c2, d); }
                SBAR();
                o = MFMA(v1p, __builtin_bit_cast(bf16x8, p1), o);
                SBAR();
                v1p = v1;
                v1 = lds_b128(vrd1 + i * 1040);
                SBAR();
                { float a = EXP2(s[4]), b2 = EXP2(s[5]), c2 = EXP2(s[6]), d = EXP2(s[7]); p0[2] = cvtpk(a, b2); p0[3] = cvtpk(c2, d); }
                SBAR();
                float e8 = EXP2(s[8]), e9 = EXP2(s[9]);
                SBAR();
                o = MFMA(v0, __builtin_bit_cast(bf16x8, p0), o);
                SBAR();
                v0 = nv0;
                { float c2 = EXP2(s[10]), d = EXP2(s[11]); p1[0] = cvtpk(e8, e9); p1[1] = cvtpk(c2, d); }
                SBAR();
                if (ph < 2 && fon) {
                    acc[ph] = MFMA(wf, ha[e], acc[ph]);
                    acc[2 + ph] = MFMA(wf, hb[e], acc[2 + ph]);
                }
                SBAR();
                { float a = EXP2(s[12]), b2 = EXP2(s[13]), c2 = EXP2(s[14]), d = EXP2(s[15]); p1[2] = cvtpk(a, b2); p1[3] = cvtpk(c2, d); }
                SBAR();
                if ((ph == 2 || i == 7) && c >= 3) {
                    const int k = min(fk0 + j0 + 3 + e, fk0 + fnk - 1);
                    const char* src = c < 7 ? (const char*)wd + (size_t)k * 16384 : (const char*)wo + (16 + 2 * wid + (e & 1)) * 16384;
                    asm volatile("s_waitcnt lgkmcnt(0)" ::: "memory");
                    glds16(src, doff, lds0 + wid * RING + e * 2048);
                    glds16(src + 8192, doff, lds0 + wid * RING + e * 2048 + 1024);
                    gld16_sc1_tied(ha[e], hfa + k * 1024, loff);
                    gld16_sc1_tied(hb[e], hfb + k * 1024, loff);
                }
                SBAR();
                s = sn;
            }
            krd += CHK; vrd0 += CHK; vrd1 += CHK;
        }
        o = MFMA(v1p, __builtin_bit_cast(bf16x8, p1), o);
        asm volatile("s_waitcnt vmcnt(0)" : "+v"(ha[0]), "+v"(hb[0]), "+v"(ha[1]), "+v"(hb[1]), "+v"(ha[2]), "+v"(hb[2]) :: "memory");
    }
    STAMP(40);
    {
        float chk = 0.f;
#pragma unroll
        for (int i = 0; i < 8; ++i) chk += fabsf(o[i]) * 0.125f;
        const bool bad = !(o[8] <= 1.8446744e19f) || !(chk <= 3.0e38f);
        if (__builtin_expect(__any(bad), 0)) o = attn_fallback(kf, vf, qf, lane);
        const float rl = agate * __builtin_amdgcn_rcpf(o[8]);
        gst16_sc1(ws + WS_O + ((size_t)AT * NH + head) * 1024, loff, pack8(o, 0, rl));
        wait_vm<0>();
        bar();
        if (tid < 8) __hip_atomic_store((gu64*)(ws + WS_FO + tid * 2048 + qb * 256) + head, tag, RLX_AGENT);
    }
    STAMP(41);
    LDSP f32x4* xch = (LDSP f32x4*)((ldsptr)smem + XCH);
#pragma unroll
    for (int t = 0; t < 2; ++t)
#pragma unroll
        for (int i = 0; i < 4; ++i) {
            f32x4 v = {acc[2 + t][4 * i], acc[2 + t][4 * i + 1], acc[2 + t][4 * i + 2], acc[2 + t][4 * i + 3]};
            xch[wid * 512 + (t * 4 + i) * 64 + lane] = v;
        }
    bf16x8 wa[8][2];
    {
        const int base = kq < 2 ? WO_LO + kq * 16384 : (kq - 2) * 4 * RING, st2 = kq < 2 ? 4096 : RING;
#pragma unroll
        for (int j = 0; j < 8; ++j) {
            ldsptr p = (ldsptr)smem + base + (j >> 1) * st2 + (j & 1) * 2048;
            wa[j][0] = tr_frag(p + wfo0, p + wfo0 + 1024);
            wa[j][1] = tr_frag(p + wfo1, p + wfo1 + 1024);
        }
    }
    if (wid == 0) spin_flags(ws + WS_FO + nb * 2048 + (mb >> 2) * 256 + r * 8, true, tag);
    if (wid == 1 && blockIdx.x == 0) {
#pragma unroll
        for (int i = 0; i < 4; ++i) spin_flags(ws + WS_FQ + i * 512 + lane * 8, true, tag);
        if (lane == 0) __hip_atomic_store((gu64*)(ws + WS_EP), tag, RLX_AGENT);
    }
    bar();
    STAMP(42);
    const char* of = ws + WS_O + ((size_t)(2 * mb + mh) * NH + kq * 8) * 1024;
    bf16x8 oa[8];
#pragma unroll
    for (int j = 0; j < 8; ++j) gld16_sc1(oa[j], of + j * 1024, loff);
#pragma unroll
    for (int t = 0; t < 2; ++t)
#pragma unroll
        for (int i = 0; i < 4; ++i) {
            const f32x4 v = xch[(wid ^ 1) * 512 + (t * 4 + i) * 64 + lane];
            acc[t][4 * i] += v[0]; acc[t][4 * i + 1] += v[1]; acc[t][4 * i + 2] += v[2]; acc[t][4 * i + 3] += v[3];
        }
#pragma unroll
    for (int j = 0; j < 8; ++j) {
        if (j == 0) wait_vm1<7>(oa[j]); else if (j == 1) wait_vm1<6>(oa[j]); else if (j == 2) wait_vm1<5>(oa[j]);
        else if (j == 3) wait_vm1<4>(oa[j]); else if (j == 4) wait_vm1<3>(oa[j]); else if (j == 5) wait_vm1<2>(oa[j]);
        else if (j == 6) wait_vm1<1>(oa[j]); else wait_vm1<0>(oa[j]);
        acc[0] = MFMA(wa[j][0], oa[j], acc[0]);
        acc[1] = MFMA(wa[j][1], oa[j], acc[1]);
        SBAR();
    }
    STAMP(43);
#pragma unroll
    for (int t = 0; t < 2; ++t)
#pragma unroll
        for (int i = 0; i < 4; ++i) {
            f32x4 v = {acc[t][4 * i], acc[t][4 * i + 1], acc[t][4 * i + 2], acc[t][4 * i + 3]};
            xch[(wid ^ 1) * 512 + (t * 4 + i) * 64 + lane] = v;
        }
    bar();
    f32x4 ra = {}, rb4 = {};
#pragma unroll
    for (int k = 0; k < 4; ++k) {
        ra += xch[((2 * k + mh) ^ 1) * 512 + (kq * 2) * 64 + lane];
        rb4 += xch[((2 * k + mh) ^ 1) * 512 + (kq * 2 + 1) * 64 + lane];
    }
    const u32x4 ov = {cvtpk(ra[0], ra[1]), cvtpk(ra[2], ra[3]), cvtpk(rb4[0], rb4[1]), cvtpk(rb4[2], rb4[3])};
    *(u32x4*)((char*)out + (size_t)(mb * 64 + mh * 32 + r) * 1024 + (nb * 64 + (kq >> 1) * 32 + h * 16 + (kq & 1) * 8) * 2) = ov;
}

extern "C" void kernel_launch(void* const* d_in, const int* in_sizes, int n_in, void* d_out, int out_size, void* d_ws,
                              size_t ws_size, hipStream_t stream) {
    (void)hipFuncSetAttribute((const void*)fused, hipFuncAttributeMaxDynamicSharedMemorySize, LDS_BYTES);
    fused<<<256, 512, LDS_BYTES, stream>>>((const u16*)d_in[0], (const u16*)d_in[1], (const u16*)d_in[2], (const u16*)d_in[4],
                                           (const u16*)d_in[5], (const u16*)d_in[6], (const u16*)d_in[7], (char*)d_ws, (u16*)d_out);
}
```

```cpp
#include <hip/hip_runtime.h>
#include <hip/hip_bf16.h>
#include <stdint.h>
#include <type_traits>

typedef unsigned short u16;
typedef unsigned long long u64;
typedef __attribute__((ext_vector_type(8))) __bf16 bf16x8;
typedef __attribute__((ext_vector_type(2))) __bf16 bf16x2;
typedef __attribute__((ext_vector_type(4))) short s16x4;
typedef __attribute__((ext_vector_type(16))) float f32x16;
typedef __attribute__((ext_vector_type(4))) float f32x4;
typedef __attribute__((ext_vector_type(2))) float f32x2;
typedef __attribute__((ext_vector_type(4))) unsigned u32x4;
#define LDSP __attribute__((address_space(3)))
typedef LDSP char* ldsptr;
typedef __attribute__((address_space(1))) unsigned gu32;
typedef __attribute__((address_space(1))) u64 gu64;

constexpr int S_ = 2048, F_ = 1376, NH = 32;
constexpr int NTT = S_ / 32;
constexpr int NKF = F_ / 16;
constexpr float LOG2E = 1.4426950408889634f;
constexpr float QSCALE = 0.25f * LOG2E;

constexpr size_t WS_EP = 0;
constexpr size_t WS_FQ = 1024;
constexpr size_t WS_FH = WS_FQ + 2048;
constexpr size_t WS_FO = WS_FH + 2048;
constexpr size_t WS_G = WS_FO + 16384;
constexpr size_t WS_Q = 262144;
constexpr size_t WS_K = WS_Q + (size_t)NH * NTT * 1024;
constexpr size_t WS_V = WS_K + (size_t)NH * NTT * 1024;
constexpr size_t WS_O = WS_V + (size_t)NH * NTT * 1024;
constexpr size_t WS_H = WS_O + (size_t)NTT * 32 * 1024;

constexpr size_t WS_DUMP = WS_H + (size_t)NTT * NKF * 1024;

constexpr int LDS_BYTES = 163840;
constexpr int SLOT = 28672;
__host__ __device__ constexpr int phys(int st) { return 20480 + ((st + 4) % 5) * SLOT; }
constexpr int F1OFF = 4064, F1UP = 12320;
constexpr int CHK = 16512, KOFF = 8320;
constexpr int WO_LO = 130048, POLL = 162816, XCH = 49536, RING = 6144;

#define MFMA(a, b, c) __builtin_amdgcn_mfma_f32_32x32x16_bf16((a), (b), (c), 0, 0, 0)
#define OUTM 0
#define NODMA 0
#define NOGATE 0
#define ROT 1
#define KROT 1
#define IBAR 4
#define POLLG 18
#define SPIN(x) asm volatile("" : "+s"(x))
#define SBAR() __builtin_amdgcn_sched_barrier(0)
#define EXP2(x) __builtin_amdgcn_exp2f(x)
#define RLX_AGENT __ATOMIC_RELAXED, __HIP_MEMORY_SCOPE_AGENT
#define STAMP(i)
#define STAMPC(i)

__device__ __forceinline__ unsigned cvtpk(float lo, float hi) {
    f32x2 v = {lo, hi};
    return __builtin_bit_cast(unsigned, __builtin_convertvector(v, bf16x2));
}
__device__ __forceinline__ void glds16(const void* sbase, unsigned voff, unsigned lds_s, int lds_i) {
    asm volatile("s_add_u32 m0, %2, %3\n\ts_nop 0\n\tglobal_load_lds_dwordx4 %0, %1" :: "v"(voff), "s"(sbase), "s"(lds_s), "i"(lds_i) : "memory", "scc");
}
__device__ __forceinline__ void glds16_sc1(const void* sbase, unsigned voff, unsigned lds_s, int lds_i) {
    asm volatile("s_add_u32 m0, %2, %3\n\ts_nop 0\n\tglobal_load_lds_dwordx4 %0, %1 sc1" :: "v"(voff), "s"(sbase), "s"(lds_s), "i"(lds_i) : "memory", "scc");
}
__device__ __forceinline__ void glds4_sc1(const void* sbase, unsigned voff, unsigned lds_s, int lds_i) {
    asm volatile("s_add_u32 m0, %2, %3\n\ts_nop 0\n\tglobal_load_lds_dword %0, %1 sc1" :: "v"(voff), "s"(sbase), "s"(lds_s), "i"(lds_i) : "memory", "scc");
}
__device__ __forceinline__ void gld16_sc1(bf16x8& r, const void* sbase, unsigned voff) {
    asm volatile("global_load_dwordx4 %0, %1, %2 sc1" : "=v"(r) : "v"(voff), "s"(sbase) : "memory");
}
__device__ __forceinline__ void gld16_sc1_tied(bf16x8& r, const void* sbase, unsigned voff) {
    asm volatile("global_load_dwordx4 %0, %1, %2 sc1" : "+v"(r) : "v"(voff), "s"(sbase) : "memory");
}
__device__ __forceinline__ void gld4_sc1(float& r, const void* sbase, unsigned voff) {
    asm volatile("global_load_dword %0, %1, %2 sc1" : "=v"(r) : "v"(voff), "s"(sbase) : "memory");
}
__device__ __forceinline__ void gst16_sc1(void* sbase, unsigned voff, bf16x8 v) {
    asm volatile("global_store_dwordx4 %0, %1, %2 sc1\n\ts_nop 1" :: "v"(voff), "v"(v), "s"(sbase) : "memory");
}
__device__ __forceinline__ void gst4_sc1(void* sbase, unsigned voff, float v) {
    asm volatile("global_store_dword %0, %1, %2 sc1" :: "v"(voff), "v"(v), "s"(sbase) : "memory");
}
template <int N> __device__ __forceinline__ void wait_vm() { asm volatile("s_waitcnt vmcnt(%0)" ::"i"(N) : "memory"); }
template <int N> __device__ __forceinline__ void wait_vm1(bf16x8& a) { asm volatile("s_waitcnt vmcnt(%1)" : "+v"(a) : "i"(N) : "memory"); }
template <int N> __device__ __forceinline__ void wait_vm2(bf16x8& a, bf16x8& b) {
    asm volatile("s_waitcnt vmcnt(%2)" : "+v"(a), "+v"(b) : "i"(N) : "memory");
}
__device__ __forceinline__ void bar() { asm volatile("s_waitcnt lgkmcnt(0)\n\ts_barrier" ::: "memory"); }
__device__ __forceinline__ ldsptr ladd(ldsptr p, int off) {
    unsigned r;
    asm volatile("v_add_u32 %0, %1, %2" : "=v"(r) : "i"(off), "v"((unsigned)(uintptr_t)p));
    return (ldsptr)(uintptr_t)r;
}
__device__ __forceinline__ bf16x8 lds_b128(ldsptr p) { return *(LDSP bf16x8*)p; }
__device__ __forceinline__ bf16x8 tr_frag(ldsptr p0, ldsptr p1) {
    s16x4 a = __builtin_amdgcn_ds_read_tr16_b64_v4i16((LDSP s16x4*)p0), b = __builtin_amdgcn_ds_read_tr16_b64_v4i16((LDSP s16x4*)p1);
    typedef __attribute__((ext_vector_type(8))) short s16x8;
    s16x8 v = {a[0], a[1], a[2], a[3], b[0], b[1], b[2], b[3]};
    return __builtin_bit_cast(bf16x8, v);
}
__device__ __forceinline__ bf16x8 pack8(const f32x16& a, int o, float sc) {
    u32x4 v = {cvtpk(a[o] * sc, a[o + 1] * sc), cvtpk(a[o + 2] * sc, a[o + 3] * sc),
               cvtpk(a[o + 4] * sc, a[o + 5] * sc), cvtpk(a[o + 6] * sc, a[o + 7] * sc)};
    return __builtin_bit_cast(bf16x8, v);
}
__device__ __forceinline__ bf16x8 pack8(const f32x16& a, int o) {
    u32x4 v = {cvtpk(a[o], a[o + 1]), cvtpk(a[o + 2], a[o + 3]), cvtpk(a[o + 4], a[o + 5]), cvtpk(a[o + 6], a[o + 7])};
    return __builtin_bit_cast(bf16x8, v);
}
__device__ __forceinline__ void spin_flags(const char* p, bool need, u64 tag) {
#pragma unroll 1
    for (int spin = 0; spin < (1 << 20); ++spin) {
        const u64 v = __hip_atomic_load((gu64*)p, RLX_AGENT);
        if (__all(!need || v == tag)) break;
        __builtin_amdgcn_s_sleep(2);
    }
}

__host__ __device__ constexpr int grp(int s, int nq) { return s <= 7 ? nq : 3; }
__host__ __device__ constexpr int younger(int s, int nq) {
    int n = 0;
    for (int t = s + 1; t <= (s == 0 ? 4 : s + 3); ++t) n += grp(t, nq);
    return n;
}
template <int NQ> __device__ __forceinline__ void wait_stage(int s) {
    if (s == 1) wait_vm<younger(1, NQ)>(); else if (s == 2) wait_vm<younger(2, NQ)>();
    else if (s == 3) wait_vm<younger(3, NQ)>(); else if (s == 4) wait_vm<younger(4, NQ)>();
    else if (s == 5) wait_vm<younger(5, NQ)>(); else if (s == 6) wait_vm<younger(6, NQ)>();
    else if (s == 7) wait_vm<younger(7, NQ)>(); else wait_vm<younger(8, NQ)>();
}
__device__ __forceinline__ void flag_store(void* sbase, unsigned voff, u64 v, unsigned mask) {
    asm volatile("s_mov_b32 exec_lo, %3\n\ts_mov_b32 exec_hi, 0\n\tglobal_store_dwordx2 %0, %1, %2 sc1\n\ts_mov_b64 exec, -1" :: "v"(voff), "v"(v), "s"(sbase), "s"(mask) : "memory");
}
#define PIN(x) asm volatile("" : "+v"(x))

__device__ __forceinline__ f32x16 attn_fallback(const char* kf, const char* vf, bf16x8 qf, int lane) {
    f32x16 o = {};
    float m = -INFINITY;
    const bool isv = ((lane >> 4) & 1) == 0;
    const u32x4 ones = {0x3f803f80u, 0x3f803f80u, 0x3f803f80u, 0x3f803f80u};
    const unsigned voff = (lane >> 5) * 256 + (lane & 15) * 16;
#pragma unroll 1
    for (int t = 0; t < NTT; ++t) {
        bf16x8 kk, a0, a1;
        gld16_sc1(kk, kf + t * 1024, lane * 16);
        gld16_sc1(a0, vf + t * 1024, voff);
        gld16_sc1(a1, vf + t * 1024 + 512, voff);
        wait_vm1<0>(kk);
        wait_vm2<0>(a0, a1);
        u32x4 v0 = __builtin_bit_cast(u32x4, a0), v1 = __builtin_bit_cast(u32x4, a1);
        if (!isv) { v0 = ones; v1 = ones; }
        f32x16 z = {};
        f32x16 s = MFMA(kk, qf, z);
        float tm = s[0];
#pragma unroll
        for (int i = 1; i < 16; ++i) tm = fmaxf(tm, s[i]);
        tm = fmaxf(tm, __shfl_xor(tm, 32));
        const float mn = fmaxf(m, tm);
        const float al = EXP2(m - mn);
        m = mn;
#pragma unroll
        for (int i = 0; i < 16; ++i) { o[i] *= al; s[i] = EXP2(s[i] - mn); }
        o = MFMA(__builtin_bit_cast(bf16x8, v0), pack8(s, 0), o);
        o = MFMA(__builtin_bit_cast(bf16x8, v1), pack8(s, 8), o);
    }
    return o;
}

__global__ __launch_bounds__(512, 2) void fused(const u16* __restrict__ x, const u16* __restrict__ wqkv, const u16* __restrict__ wo,
                                                const u16* __restrict__ wg, const u16* __restrict__ wu, const u16* __restrict__ wd,
                                                const u16* __restrict__ gatew, char* __restrict__ ws, u16* __restrict__ out) {
    extern __shared__ __attribute__((aligned(16))) char smem[];
    STAMP(0); STAMPC(61);
    const int tid = threadIdx.x, lane = tid & 63;
    const int wid = __builtin_amdgcn_readfirstlane(tid >> 6);
    const int xcd = blockIdx.x & 7, idx = blockIdx.x >> 3;
    const unsigned lds0 = 0;
    const unsigned loff = lane * 16;
    const int r = lane & 31, h = lane >> 5, q = (lane & 15) >> 2, p4 = lane & 3, blk = (lane >> 4) & 1;
    const int rb = (xcd & 1) * 8 + (idx & 7), cg = (xcd >> 1) * 4 + (idx >> 3);
    const int tt = wid & 3, ch = wid >> 2, hg = cg >> 1, odd = cg & 1;
    const int rot = ROT * ((idx & 7) + 2 * (idx >> 3));
    unsigned xoff[2], woff[2], foff[3];
#pragma unroll
    for (int e = 0; e < 2; ++e) {
        const int row = wid * 16 + e * 8 + (lane >> 3);
        xoff[e] = (unsigned)((rb * 128 + row) * 1024 + (((lane & 7) ^ ((row >> 1) & 7)) * 16));
    }
    const int wp0 = ch ? 2 * wid - 4 : wid;
#pragma unroll
    for (int e = 0; e < 2; ++e) {
        const int n = (wp0 + e) * 64 + lane, kr = n / 12, c = n % 12;
        woff[e] = (unsigned)(kr * 3072 + hg * 128 + (c < 8 ? odd * 2048 + c * 16 : 1024 - 128 + odd * 64 + c * 16));
    }
    unsigned ldx = lds0 + wid * 2048, ldw = lds0 + wp0 * 1024;
    SPIN(ldx); SPIN(ldw);
    auto issue_q = [&](int st, int op) {
        const int kb = (st + rot) & 7;
        if (op == 0) glds16((const char*)x + kb * 128, xoff[0], ldx, phys(st));
        if (op == 1) glds16((const char*)wqkv + kb * (64 * 3072), woff[0], ldw, phys(st) + 16384);
        if (op == 2) glds16((const char*)x + kb * 128, xoff[1], ldx, phys(st) + 1024);
        if (op == 3) glds16((const char*)wqkv + kb * (64 * 3072), woff[1], ldw, phys(st) + 16384 + 1024);
    };
    if (wid == 3) glds16(gatew, (loff + rot * 128) & 1023, lds0, 0);
#pragma unroll
    for (int st = 0; st < 5; ++st) {
        issue_q(st, 0); issue_q(st, 1); issue_q(st, 2);
        if (ch) issue_q(st, 3);
        if (st < IBAR) asm volatile("s_barrier" ::: "memory");
    }
    STAMP(1);
    const u64 ep = *(const __attribute__((address_space(4))) u64*)(uintptr_t)(ws + WS_EP);
    const u64 tag = (ep ^ (ep >> 31)) * 0x9E3779B97F4A7C15ull + 0xD1B54A32D192ED03ull;
    unsigned lead = __builtin_amdgcn_readfirstlane(tid < 64 ? 1 : 0);
    SPIN(lead);
    const int u0 = cg < 14 ? cg * 6 : 80;
#pragma unroll
    for (int e = 0; e < 3; ++e) {
        const int n = (tt * 3 + e) * 64 + lane, kr = n / 12, c = n % 12;
        foff[e] = (unsigned)(kr * (F_ * 2) + u0 * 32 + c * 16);
    }
    const char* fsrc = (const char*)(ch ? wu : wg);
    unsigned fdst = lds0 + F1OFF + ch * F1UP + tt * 3072;
    SPIN(fdst);
    auto issue_f = [&](int st, int op) {
        if (op < 3) glds16(fsrc + ((st + rot) & 7) * (64 * F_ * 2), foff[op], fdst, phys(st) + op * 1024);
    };
    bf16x8 xr[32];
    const int T = rb * 4 + tt;
    bf16x8 fw[3][3];
    ldsptr frd = (ldsptr)smem + F1OFF + (8 * h + q) * 192 + blk * F1UP + p4 * 8 + ch * 96, fs_;
    auto frd_ = [&](int g) {
        if ((g & 3) == 0) fs_ = ladd(frd, phys(8 + (g >> 2)));
        const int so = (g & 3) * 3072;
#pragma unroll
        for (int u = 0; u < 3; ++u) fw[g % 3][u] = tr_frag(fs_ + so + u * 32, fs_ + so + u * 32 + 768);
    };
    {
        ldsptr xrd[4];
#pragma unroll
        for (int ks = 0; ks < 4; ++ks) xrd[ks] = (ldsptr)smem + (tt * 32 + r) * 128 + (((2 * ks + h) ^ ((r >> 1) & 7)) * 16);
        ldsptr wrd = (ldsptr)smem + 16384 + (8 * h + q) * 192 + blk * 32 + p4 * 8 + ch * 128;
        ldsptr grd = (ldsptr)smem + h * 16;
        auto qkv = [&](auto var) {
            constexpr int VAR = decltype(var)::value;
            constexpr int NQ = VAR == 2 ? 4 : 3;
            f32x16 a0 = {}, a1 = {};
            float gacc = 0.f;
            bf16x8 w0[3], w1[3];
            ldsptr ws_;
            auto rd = [&](int g) {
                const int so = phys(g >> 2), ks = g & 3;
                if (ks == 0) ws_ = ladd(wrd, so);
                xr[g] = lds_b128(ladd(xrd[ks], so));
                w0[g % 3] = tr_frag(ws_ + ks * 3072, ws_ + ks * 3072 + 768);
                if (VAR != 2) w1[g % 3] = tr_frag(ws_ + ks * 3072 + 64, ws_ + ks * 3072 + 64 + 768);
                else w1[g % 3] = lds_b128(grd + g * 32);
            };
            wait_vm<younger(0, NQ)>();
            bar();
            STAMP(2);
            rd(0);
            rd(1);
#pragma unroll
            for (int g = 0; g < 32; ++g) {
                const int s = (g + 2) >> 2, op = (g + 2) & 3;
                if (op == 0) {
                    wait_stage<NQ>(s);
                    bar();
                    STAMP(2 + s);
                }
                if (g < 30) rd(g + 2); else frd_(g - 30);
                SBAR();
                if (VAR == 1) { a0 = MFMA(xr[g], w0[g % 3], a0); a1 = MFMA(xr[g], w1[g % 3], a1); }
                else { a0 = MFMA(w0[g % 3], xr[g], a0); if (VAR == 0) a1 = MFMA(w1[g % 3], xr[g], a1); }
                PIN(a0); if (VAR != 2) PIN(a1);
                SBAR();
                if (s >= 1 && !NODMA) { if (s + 4 <= 7) { if (op < NQ) issue_q(s + 4, op); } else issue_f(s + 4, op); }
                if (VAR == 2 && !NOGATE) {
#pragma unroll
                    for (int j = 0; j < 4; ++j) {
                        const bf16x2 xa = {xr[g][2 * j], xr[g][2 * j + 1]}, ga = {w1[g % 3][2 * j], w1[g % 3][2 * j + 1]};
                        gacc = __builtin_amdgcn_fdot2_f32_bf16(xa, ga, gacc, false);
                    }
                    PIN(gacc);
                }
                SBAR();
            }
            STAMP(11);
            if (VAR == 0) {
                char* qp = ws + WS_Q + ((size_t)(4 * hg) * NTT + T) * 1024;
                gst16_sc1(qp, loff, pack8(a0, 0, QSCALE));
                gst16_sc1(qp + NTT * 1024, loff, pack8(a0, 8, QSCALE));
                gst16_sc1(qp + 2 * NTT * 1024, loff, pack8(a1, 0, QSCALE));
                gst16_sc1(qp + 3 * NTT * 1024, loff, pack8(a1, 8, QSCALE));
            } else if (VAR == 1) {
                char* vp = ws + WS_V + ((size_t)(4 * hg) * NTT + T) * 1024;
                const unsigned vo = blk * (NTT * 1024) + h * 256 + (lane & 15) * 16;
                gst16_sc1(vp, vo, pack8(a0, 0));
                gst16_sc1(vp + 512, vo, pack8(a0, 8));
                gst16_sc1(vp + 2 * NTT * 1024, vo, pack8(a1, 0));
                gst16_sc1(vp + 2 * NTT * 1024 + 512, vo, pack8(a1, 8));
            } else {
                char* kp = ws + WS_K + ((size_t)(4 * hg + 2 * odd) * NTT + T) * 1024;
                gst16_sc1(kp, loff, pack8(a0, 0));
                gst16_sc1(kp + NTT * 1024, loff, pack8(a0, 8));
                gacc += __shfl_xor(gacc, 32);
                gst4_sc1(ws + WS_G + (cg * S_ + T * 32) * 4, r * 4, __builtin_amdgcn_rcpf(1.0f + EXP2(-LOG2E * gacc)));
            }
        };
        if (ch) qkv(std::integral_constant<int, 2>{});
        else if (odd) qkv(std::integral_constant<int, 1>{});
        else qkv(std::integral_constant<int, 0>{});
    }
    const int head = xcd * 4 + (idx & 3), qb = idx >> 2;
    const int AT = qb * 8 + wid;
    const int krot = KROT * 8 * qb;
    const char* kf = ws + WS_K + (size_t)head * NTT * 1024;
    const char* vf = ws + WS_V + (size_t)head * NTT * 1024;
    const bool isv = blk == 0;
    const unsigned voff = h * 256 + (lane & 15) * 16;
    const u32x4 ones = {0x3f803f80u, 0x3f803f80u, 0x3f803f80u, 0x3f803f80u};
    const int mb = xcd * 4 + (idx & 3), nb = idx >> 2;
    const int mh = wid & 1, kq = wid >> 1;
    bf16x8 qf, k0, kk, g0, g1;
    float agate, tgate;
    unsigned ldv = lds0 + wid * 1040, ldk = lds0 + wid * 1024;
    SPIN(ldv); SPIN(ldk);
    auto issue_kv = [&](int c, int op) {
        if (op == 0) glds16_sc1(vf + ((8 * c + 1 + wid + krot) & 63) * 1024, loff, ldv, c * CHK);
        else glds16_sc1(kf + ((8 * c + 2 + wid + krot) & 63) * 1024, loff, ldk, c * CHK + KOFF);
    };
    f32x16 o, acc[4];
    bf16x8 ha[3], hb[3];
    {
        f32x16 fa[3] = {};
        u64 pollv = 0;
        const char* fq = ws + WS_FQ + xcd * 256;
        STAMP(12);
#pragma unroll
        for (int g = 0; g < 32; ++g) {
            const int s = 8 + ((g + 2) >> 2), op = (g + 2) & 3;
            if (op == 0 && s <= 14) {
                if (s == 9) wait_vm<12>();
                else if (s == 10) wait_vm<4>();
                else if (s == 12) wait_vm<9>();
                else if (s == 13) wait_vm<6>();
                else if (s == 14) {
                    asm volatile("s_waitcnt vmcnt(0)" : "+v"(pollv) :: "memory");
                    if (wid == 0 && !__all(pollv == tag)) spin_flags(fq + r * 8, true, tag);
                }
                bar();
                STAMP(4 + s);
                if (s == 10) flag_store(ws + WS_FQ + hg * 256 + (odd * 16 + rb) * 8, 0, tag, lead);
            }
            if (g < 30) frd_(g + 2);
            SBAR();
#pragma unroll
            for (int u = 0; u < 3; ++u) fa[u] = MFMA(fw[g % 3][u], xr[g], fa[u]);
            PIN(fa[0]); PIN(fa[1]); PIN(fa[2]);
            SBAR();
            if (s <= 11 && !NODMA) issue_f(s + 4, op);
            if (g == POLLG) asm volatile("global_load_dwordx2 %0, %1, %2 sc1" : "+v"(pollv) : "v"(r * 8), "s"(fq) : "memory");
            if (g == 22) { gld16_sc1(qf, ws + WS_Q + ((size_t)head * NTT + AT) * 1024, loff); gld16_sc1(k0, kf + krot * 1024, loff); }
            if (g == 23) { gld16_sc1(g0, vf + krot * 1024, voff); gld16_sc1(g1, vf + krot * 1024 + 512, voff); }
            if (g == 24) { gld16_sc1(kk, kf + (krot + 1) * 1024, loff); issue_kv(0, 0); }
            if (g == 25) { issue_kv(0, 1); gld4_sc1(agate, ws + WS_G + (2 * xcd * S_ + AT * 32) * 4, r * 4); }
            if (g >= 26) issue_kv(1 + (g - 26) / 2, g & 1);
            if (g == 26) gld4_sc1(tgate, ws + WS_G + (2 * xcd * S_ + (2 * mb + mh) * 32) * 4, r * 4);
            if (g == 22) *(LDSP u32x4*)((ldsptr)smem + min(wid, 5) * CHK + (lane & 7) * 1040 + 1024) = ones;
            if (g >= 24 && g < 28) { acc[g - 24] = f32x16{}; PIN(acc[g - 24]); }
            if (g == 28) { o = f32x16{}; PIN(o); }
            if (g >= 29) { ha[g - 29] = bf16x8{}; hb[g - 29] = bf16x8{}; PIN(ha[g - 29]); PIN(hb[g - 29]); }
            SBAR();
        }
        STAMP(20);
        char* hp = ws + WS_H + ((size_t)T * NKF + u0 + ch * 3) * 1024;
#pragma unroll
        for (int u = 0; u < 3; ++u) {
            f32x16 t;
#pragma unroll
            for (int j = 0; j < 8; ++j) {
                const float g = fa[u][j];
                t[j] = g * fa[u][8 + j] * __builtin_amdgcn_rcpf(1.0f + EXP2(-LOG2E * g));
            }
            const bool own = cg < 14 || (cg == 14 && ch * 3 + u >= 4);
            gst16_sc1(own ? hp + u * 1024 : ws + WS_DUMP + (blockIdx.x * 8 + wid) * 1024, loff, pack8(t, 0));
        }
    }
    const int fk0 = wid * 11 - (wid > 6 ? 1 : 0) - (wid < 6 ? 1 : 2);
    unsigned doff;
    {
        const int row = lane >> 3;
        doff = (unsigned)(row * 1024 + nb * 128 + (((lane & 7) ^ (((row >> 1) & 1) << 2)) * 16));
    }
    const int row0 = 4 * h + q, sw = (row0 >> 1) & 1;
    const int wfo0 = row0 * 128 + sw * 64 + (p4 & 1) * 32 + blk * 16 + (p4 >> 1) * 8, wfo1 = wfo0 ^ 64;
    const char* hfa = ws + WS_H + ((ptrdiff_t)(2 * mb + mh) * NKF + fk0) * 1024;
    const char* hfb = ws + WS_H + ((ptrdiff_t)(2 * mb + 1 - mh) * NKF + fk0) * 1024;
    const char* wdp = (const char*)wd + (ptrdiff_t)fk0 * 16384;
    {
        asm volatile("s_waitcnt vmcnt(11)" : "+v"(qf), "+v"(k0), "+v"(kk), "+v"(g0), "+v"(g1) :: "memory");
        STAMP(21);
        f32x16 z = {};
        f32x16 s = MFMA(k0, qf, z);
        bar();
        float m = fmaxf(s[0], s[1]);
#pragma unroll
        for (int i = 2; i < 16; i += 2) m = fmaxf(fmaxf(m, s[i]), s[i + 1]);
        m = fmaxf(m, __shfl_xor(m, 32));
        f32x16 negm;
#pragma unroll
        for (int i = 0; i < 16; ++i) negm[i] = -m;
        s = MFMA(k0, qf, negm);
        bf16x8 v0 = isv ? g0 : __builtin_bit_cast(bf16x8, ones), v1 = isv ? g1 : __builtin_bit_cast(bf16x8, ones);
        u32x4 p1 = {0, 0, 0, 0};
        bf16x8 v1p = v1;
        ldsptr krd = (ldsptr)smem + KOFF + lane * 16;
        ldsptr vrd0 = (ldsptr)smem + (isv ? voff : 1024);
        ldsptr vrd1 = (ldsptr)smem + (isv ? 512 + voff : 1024);
        ldsptr wr0 = (ldsptr)smem + wid * RING + wfo0, wr1 = (ldsptr)smem + wid * RING + wfo1;
        unsigned ringb = lds0 + wid * RING;
        SPIN(ringb);
        auto refill = [&](int e, const char* wsrc, int n, u64 mask) {
            glds16(wsrc, doff, ringb, e * 2048);
            glds16(wsrc + 8192, doff, ringb, e * 2048 + 1024);
            asm volatile("s_mov_b64 exec, %5\n\tglobal_load_dwordx4 %0, %2, %3 sc1\n\tglobal_load_dwordx4 %1, %2, %4 sc1\n\ts_mov_b64 exec, -1"
                         : "+v"(ha[e]), "+v"(hb[e]) : "v"(loff), "s"(hfa + n * 1024), "s"(hfb + n * 1024), "s"(mask) : "memory");
        };
        auto chunk = [&](auto fused_, const char* ws0, int wst0, int wst, int n0, u64 m0_, u64 m1_, u64 m2_) {
            constexpr bool FU = decltype(fused_)::value;
            bf16x8 wf;
#pragma unroll
            for (int i = 0; i < 8; ++i) {
                const int e = i / 3, ph = i % 3;
                SBAR();
                f32x16 sn = MFMA(kk, qf, negm);
                SBAR();
                kk = lds_b128(krd + i * 1024);
                bf16x8 nv0 = lds_b128(vrd0 + i * 1040);
                if (FU && ph == 0) wait_vm2<8>(ha[e], hb[e]);
                if (FU && ph < 2) wf = tr_frag((ph ? wr1 : wr0) + e * 2048, (ph ? wr1 : wr0) + e * 2048 + 1024);
                SBAR();
                u32x4 p0;
                { float a = EXP2(s[0]), b2 = EXP2(s[1]), c2 = EXP2(s[2]), d = EXP2(s[3]); p0[0] = cvtpk(a, b2); p0[1] = cvtpk(c2, d); }
                SBAR();
                o = MFMA(v1p, __builtin_bit_cast(bf16x8, p1), o);
                SBAR();
                v1p = v1;
                v1 = lds_b128(vrd1 + i * 1040);
                SBAR();
                { float a = EXP2(s[4]), b2 = EXP2(s[5]), c2 = EXP2(s[6]), d = EXP2(s[7]); p0[2] = cvtpk(a, b2); p0[3] = cvtpk(c2, d); }
                SBAR();
                float e8 = EXP2(s[8]), e9 = EXP2(s[9]);
                SBAR();
                o = MFMA(v0, __builtin_bit_cast(bf16x8, p0), o);
                SBAR();
                v0 = nv0;
                { float c2 = EXP2(s[10]), d = EXP2(s[11]); p1[0] = cvtpk(e8, e9); p1[1] = cvtpk(c2, d); }
                SBAR();
                if (FU && ph < 2) acc[ph] = MFMA(wf, ha[e], acc[ph]);
                SBAR();
                float e12 = EXP2(s[12]), e13 = EXP2(s[13]), e14 = EXP2(s[14]), e15 = EXP2(s[15]);
                SBAR();
                if (FU && ph < 2) acc[2 + ph] = MFMA(wf, hb[e], acc[2 + ph]);
                SBAR();
                p1[2] = cvtpk(e12, e13); p1[3] = cvtpk(e14, e15);
                SBAR();
                if (FU && (ph == 2 || i == 7)) refill(e, ws0 + (e == 0 ? wst0 : e == 1 ? 16384 : wst), n0 + e, e == 0 ? m0_ : e == 1 ? m1_ : m2_);
                SBAR();
                s = sn;
            }
            krd += CHK; vrd0 += CHK; vrd1 += CHK;
        };
        STAMP(22);
#pragma unroll 1
        for (int c = 0; c < 3; ++c) {
            if (c == 0) {
                issue_kv(4, 0); issue_kv(4, 1); issue_kv(5, 0); issue_kv(5, 1);
                if (wid == 7 && lane >= 48) *(LDSP u32x4*)((ldsptr)smem + (lane >> 3) * CHK + (lane & 7) * 1040 + 1024) = ones;
            } else if (c == 1) {
                wait_vm<4>();
                bar();
                flag_store(ws + WS_FH + rb * 128 + cg * 8, 0, tag, lead);
                issue_kv(6, 0); issue_kv(6, 1);
                if (wid < 7) issue_kv(7, 0);
                if (wid < 6) issue_kv(7, 1);
            } else {
#pragma unroll
                for (int e = 0; e < 4; ++e) glds16((const char*)wo + (wid * 4 + e) * 8192, doff, ldk * 4, WO_LO + e * 1024);
                if (wid == 0) glds4_sc1(ws + WS_FH + (mb >> 1) * 128, r * 4, lds0, POLL);
            }
            STAMP(30 + c);
            chunk(std::false_type{}, nullptr, 0, 0, 0, 0, 0, 0);
        }
        wait_vm<0>();
        if (wid == 0) {
            const u64 v = *(LDSP u64*)((ldsptr)smem + POLL + (lane & 15) * 8);
            if (!__all((lane & 15) == 15 || v == tag)) spin_flags(ws + WS_FH + (mb >> 1) * 128 + (lane & 15) * 8, (lane & 15) != 15, tag);
        }
        bar();
#pragma unroll 1
        for (int c = 3; c < 8; ++c) {
            STAMP(30 + c);
            const int n0 = c < 7 ? 3 * c - 9 : 2;
            const char* w0 = c < 7 ? wdp + n0 * 16384 : (const char*)wo + (16 + 2 * wid) * 16384;
            const u64 ml = c < 7 ? ~0ull : 1ull;
            chunk(std::true_type{}, w0, c == 3 ? 16384 : 0, c < 7 ? 32768 : 0, n0, c == 3 ? 0ull : ml, c == 3 && wid >= 6 ? 0ull : ml, ml);
        }
        o = MFMA(v1p, __builtin_bit_cast(bf16x8, p1), o);
        asm volatile("s_waitcnt vmcnt(0)" : "+v"(ha[0]), "+v"(hb[0]), "+v"(ha[1]), "+v"(hb[1]), "+v"(ha[2]), "+v"(hb[2]), "+v"(agate), "+v"(tgate) :: "memory");
    }
    STAMP(40);
    {
        float chk = 0.f;
#pragma unroll
        for (int i = 0; i < 8; ++i) chk += fabsf(o[i]) * 0.125f;
        const bool bad = !(o[8] <= 1.8446744e19f) || !(chk <= 3.0e38f);
        if (__builtin_expect(__any(bad), 0)) o = attn_fallback(kf, vf, qf, lane);
        const float rl = agate * __builtin_amdgcn_rcpf(o[8]);
        gst16_sc1(ws + WS_O + ((size_t)AT * NH + head) * 1024, loff, pack8(o, 0, rl));
        wait_vm<0>();
        bar();
        if (tid < 8) __hip_atomic_store((gu64*)(ws + WS_FO + tid * 2048 + qb * 256) + head, tag, RLX_AGENT);
    }
    STAMP(41);
    LDSP f32x4* xch = (LDSP f32x4*)((ldsptr)smem + XCH);
#pragma unroll
    for (int t = 0; t < 2; ++t)
#pragma unroll
        for (int i = 0; i < 4; ++i) {
            f32x4 v = {acc[2 + t][4 * i], acc[2 + t][4 * i + 1], acc[2 + t][4 * i + 2], acc[2 + t][4 * i + 3]};
            xch[wid * 512 + (t * 4 + i) * 64 + lane] = v;
        }
    bf16x8 wa[8][2];
    {
        const int base = kq < 2 ? WO_LO + kq * 16384 : (kq - 2) * 4 * RING, st2 = kq < 2 ? 4096 : RING;
#pragma unroll
        for (int j = 0; j < 8; ++j) {
            ldsptr p = (ldsptr)smem + base + (j >> 1) * st2 + (j & 1) * 2048;
            wa[j][0] = tr_frag(p + wfo0, p + wfo0 + 1024);
            wa[j][1] = tr_frag(p + wfo1, p + wfo1 + 1024);
        }
    }
    if (wid == 0) spin_flags(ws + WS_FO + nb * 2048 + (mb >> 2) * 256 + r * 8, true, tag);
    if (wid == 1 && blockIdx.x == 0) {
#pragma unroll
        for (int i = 0; i < 4; ++i) spin_flags(ws + WS_FQ + i * 512 + lane * 8, true, tag);
        if (lane == 0) __hip_atomic_store((gu64*)(ws + WS_EP), tag, RLX_AGENT);
    }
    bar();
    STAMP(42);
    const char* of = ws + WS_O + ((size_t)(2 * mb + mh) * NH + kq * 8) * 1024;
    bf16x8 oa[8];
    const float og = 1.0f - tgate;
#pragma unroll
    for (int j = 0; j < 8; ++j) gld16_sc1(oa[j], of + j * 1024, loff);
#pragma unroll
    for (int t = 0; t < 2; ++t)
#pragma unroll
        for (int i = 0; i < 4; ++i) {
            const f32x4 v = xch[(wid ^ 1) * 512 + (t * 4 + i) * 64 + lane];
#pragma unroll
            for (int j = 0; j < 4; ++j) acc[t][4 * i + j] = (acc[t][4 * i + j] + v[j]) * og;
        }
#pragma unroll
    for (int j = 0; j < 8; ++j) {
        if (j == 0) wait_vm1<7>(oa[j]); else if (j == 1) wait_vm1<6>(oa[j]); else if (j == 2) wait_vm1<5>(oa[j]);
        else if (j == 3) wait_vm1<4>(oa[j]); else if (j == 4) wait_vm1<3>(oa[j]); else if (j == 5) wait_vm1<2>(oa[j]);
        else if (j == 6) wait_vm1<1>(oa[j]); else wait_vm1<0>(oa[j]);
        acc[0] = MFMA(wa[j][0], oa[j], acc[0]);
        acc[1] = MFMA(wa[j][1], oa[j], acc[1]);
        SBAR();
    }
    STAMP(43);
#pragma unroll
    for (int t = 0; t < 2; ++t)
#pragma unroll
        for (int i = 0; i < 4; ++i) {
            f32x4 v = {acc[t][4 * i], acc[t][4 * i + 1], acc[t][4 * i + 2], acc[t][4 * i + 3]};
            xch[(wid ^ 1) * 512 + (t * 4 + i) * 64 + lane] = v;
        }
    bar();
    f32x4 ra = {}, rb4 = {};
#pragma unroll
    for (int k = 0; k < 4; ++k) {
        ra += xch[((2 * k + mh) ^ 1) * 512 + (kq * 2) * 64 + lane];
        rb4 += xch[((2 * k + mh) ^ 1) * 512 + (kq * 2 + 1) * 64 + lane];
    }
    const u32x4 ov = {cvtpk(ra[0], ra[1]), cvtpk(ra[2], ra[3]), cvtpk(rb4[0], rb4[1]), cvtpk(rb4[2], rb4[3])};
    *(u32x4*)((char*)out + (size_t)(mb * 64 + mh * 32 + r) * 1024 + (nb * 64 + (kq >> 1) * 32 + h * 16 + (kq & 1) * 8) * 2) = ov;
}

extern "C" void kernel_launch(void* const* d_in, const int* in_sizes, int n_in, void* d_out, int out_size, void* d_ws,
                              size_t ws_size, hipStream_t stream) {
    (void)hipFuncSetAttribute((const void*)fused, hipFuncAttributeMaxDynamicSharedMemorySize, LDS_BYTES);
    fused<<<256, 512, LDS_BYTES, stream>>>((const u16*)d_in[0], (const u16*)d_in[1], (const u16*)d_in[2], (const u16*)d_in[4],
                                           (const u16*)d_in[5], (const u16*)d_in[6], (const u16*)d_in[7], (char*)d_ws, (u16*)d_out);
}
```

```cpp
#define MA 0
#define MB 2
#define MK 4
#include <hip/hip_runtime.h>
#include <hip/hip_bf16.h>
#include <stdint.h>
#include <type_traits>

typedef unsigned short u16;
typedef unsigned long long u64;
typedef __attribute__((ext_vector_type(8))) __bf16 bf16x8;
typedef __attribute__((ext_vector_type(2))) __bf16 bf16x2;
typedef __attribute__((ext_vector_type(4))) short s16x4;
typedef __attribute__((ext_vector_type(16))) float f32x16;
typedef __attribute__((ext_vector_type(4))) float f32x4;
typedef __attribute__((ext_vector_type(2))) float f32x2;
typedef __attribute__((ext_vector_type(4))) unsigned u32x4;
#define LDSP __attribute__((address_space(3)))
typedef LDSP char* ldsptr;
typedef __attribute__((address_space(1))) unsigned gu32;
typedef __attribute__((address_space(1))) u64 gu64;

constexpr int S_ = 2048, F_ = 1376, NH = 32;
constexpr int NTT = S_ / 32;
constexpr int NKF = F_ / 16;
constexpr float LOG2E = 1.4426950408889634f;
constexpr float QSCALE = 0.25f * LOG2E;

constexpr size_t WS_EP = 0;
constexpr size_t WS_FQ = 1024;
constexpr size_t WS_FH = WS_FQ + 2048;
constexpr size_t WS_FO = WS_FH + 2048;
constexpr size_t WS_G = WS_FO + 16384;
constexpr size_t WS_Q = 262144;
constexpr size_t WS_K = WS_Q + (size_t)NH * NTT * 1024;
constexpr size_t WS_V = WS_K + (size_t)NH * NTT * 1024;
constexpr size_t WS_O = WS_V + (size_t)NH * NTT * 1024;
constexpr size_t WS_H = WS_O + (size_t)NTT * 32 * 1024;

constexpr size_t WS_DUMP = WS_H + (size_t)NTT * NKF * 1024;

constexpr int LDS_BYTES = 163840;
constexpr int SLOT = 28672;
__host__ __device__ constexpr int phys(int st) { return 20480 + ((st + 4) % 5) * SLOT; }
constexpr int F1OFF = 4064, F1UP = 12320;
constexpr int CHK = 16512, KOFF = 8320;
constexpr int WO_LO = 130048, POLL = 162816, XCH = 49536, RING = 6144;

#define MFMA(a, b, c) __builtin_amdgcn_mfma_f32_32x32x16_bf16((a), (b), (c), 0, 0, 0)
#define OUTM 0
#define NODMA 0
#define NOGATE 0
#define ROT 1
#define KROT 1
#define IBAR 4
#define POLLG 18
#define SPIN(x) asm volatile("" : "+s"(x))
#define SBAR() __builtin_amdgcn_sched_barrier(0)
#define EXP2(x) __builtin_amdgcn_exp2f(x)
#define RLX_AGENT __ATOMIC_RELAXED, __HIP_MEMORY_SCOPE_AGENT
__device__ __forceinline__ u64 rt() { u64 t; asm volatile("s_memrealtime %0\n\ts_waitcnt lgkmcnt(0)" : "=s"(t) :: "memory"); return t; }
__device__ __forceinline__ u64 ct() { u64 t; asm volatile("s_memtime %0\n\ts_waitcnt lgkmcnt(0)" : "=s"(t) :: "memory"); return t; }
#define STAMP(i) do { if ((i) == MA) { SBAR(); mta = (unsigned)rt(); SBAR(); } if ((i) == MB) { SBAR(); mtb = (unsigned)rt(); SBAR(); } } while (0)
#define STAMPC(i)

__device__ __forceinline__ unsigned cvtpk(float lo, float hi) {
    f32x2 v = {lo, hi};
    return __builtin_bit_cast(unsigned, __builtin_convertvector(v, bf16x2));
}
__device__ __forceinline__ void glds16(const void* sbase, unsigned voff, unsigned lds_s, int lds_i) {
    asm volatile("s_add_u32 m0, %2, %3\n\ts_nop 0\n\tglobal_load_lds_dwordx4 %0, %1" :: "v"(voff), "s"(sbase), "s"(lds_s), "i"(lds_i) : "memory", "scc");
}
__device__ __forceinline__ void glds16_sc1(const void* sbase, unsigned voff, unsigned lds_s, int lds_i) {
    asm volatile("s_add_u32 m0, %2, %3\n\ts_nop 0\n\tglobal_load_lds_dwordx4 %0, %1 sc1" :: "v"(voff), "s"(sbase), "s"(lds_s), "i"(lds_i) : "memory", "scc");
}
__device__ __forceinline__ void glds4_sc1(const void* sbase, unsigned voff, unsigned lds_s, int lds_i) {
    asm volatile("s_add_u32 m0, %2, %3\n\ts_nop 0\n\tglobal_load_lds_dword %0, %1 sc1" :: "v"(voff), "s"(sbase), "s"(lds_s), "i"(lds_i) : "memory", "scc");
}
__device__ __forceinline__ void gld16_sc1(bf16x8& r, const void* sbase, unsigned voff) {
    asm volatile("global_load_dwordx4 %0, %1, %2 sc1" : "=v"(r) : "v"(voff), "s"(sbase) : "memory");
}
__device__ __forceinline__ void gld16_sc1_tied(bf16x8& r, const void* sbase, unsigned voff) {
    asm volatile("global_load_dwordx4 %0, %1, %2 sc1" : "+v"(r) : "v"(voff), "s"(sbase) : "memory");
}
__device__ __forceinline__ void gld4_sc1(float& r, const void* sbase, unsigned voff) {
    asm volatile("global_load_dword %0, %1, %2 sc1" : "=v"(r) : "v"(voff), "s"(sbase) : "memory");
}
__device__ __forceinline__ void gst16_sc1(void* sbase, unsigned voff, bf16x8 v) {
    asm volatile("global_store_dwordx4 %0, %1, %2 sc1\n\ts_nop 1" :: "v"(voff), "v"(v), "s"(sbase) : "memory");
}
__device__ __forceinline__ void gst4_sc1(void* sbase, unsigned voff, float v) {
    asm volatile("global_store_dword %0, %1, %2 sc1" :: "v"(voff), "v"(v), "s"(sbase) : "memory");
}
template <int N> __device__ __forceinline__ void wait_vm() { asm volatile("s_waitcnt vmcnt(%0)" ::"i"(N) : "memory"); }
template <int N> __device__ __forceinline__ void wait_vm1(bf16x8& a) { asm volatile("s_waitcnt vmcnt(%1)" : "+v"(a) : "i"(N) : "memory"); }
template <int N> __device__ __forceinline__ void wait_vm2(bf16x8& a, bf16x8& b) {
    asm volatile("s_waitcnt vmcnt(%2)" : "+v"(a), "+v"(b) : "i"(N) : "memory");
}
__device__ __forceinline__ void bar() { asm volatile("s_waitcnt lgkmcnt(0)\n\ts_barrier" ::: "memory"); }
__device__ __forceinline__ ldsptr ladd(ldsptr p, int off) {
    unsigned r;
    asm volatile("v_add_u32 %0, %1, %2" : "=v"(r) : "i"(off), "v"((unsigned)(uintptr_t)p));
    return (ldsptr)(uintptr_t)r;
}
__device__ __forceinline__ bf16x8 lds_b128(ldsptr p) { return *(LDSP bf16x8*)p; }
__device__ __forceinline__ bf16x8 tr_frag(ldsptr p0, ldsptr p1) {
    s16x4 a = __builtin_amdgcn_ds_read_tr16_b64_v4i16((LDSP s16x4*)p0), b = __builtin_amdgcn_ds_read_tr16_b64_v4i16((LDSP s16x4*)p1);
    typedef __attribute__((ext_vector_type(8))) short s16x8;
    s16x8 v = {a[0], a[1], a[2], a[3], b[0], b[1], b[2], b[3]};
    return __builtin_bit_cast(bf16x8, v);
}
__device__ __forceinline__ bf16x8 pack8(const f32x16& a, int o, float sc) {
    u32x4 v = {cvtpk(a[o] * sc, a[o + 1] * sc), cvtpk(a[o + 2] * sc, a[o + 3] * sc),
               cvtpk(a[o + 4] * sc, a[o + 5] * sc), cvtpk(a[o + 6] * sc, a[o + 7] * sc)};
    return __builtin_bit_cast(bf16x8, v);
}
__device__ __forceinline__ bf16x8 pack8(const f32x16& a, int o) {
    u32x4 v = {cvtpk(a[o], a[o + 1]), cvtpk(a[o + 2], a[o + 3]), cvtpk(a[o + 4], a[o + 5]), cvtpk(a[o + 6], a[o + 7])};
    return __builtin_bit_cast(bf16x8, v);
}
__device__ __forceinline__ void spin_flags(const char* p, bool need, u64 tag) {
#pragma unroll 1
    for (int spin = 0; spin < (1 << 20); ++spin) {
        const u64 v = __hip_atomic_load((gu64*)p, RLX_AGENT);
        if (__all(!need || v == tag)) break;
        __builtin_amdgcn_s_sleep(2);
    }
}

__host__ __device__ constexpr int grp(int s, int nq) { return s <= 7 ? nq : 3; }
__host__ __device__ constexpr int younger(int s, int nq) {
    int n = 0;
    for (int t = s + 1; t <= (s == 0 ? 4 : s + 3); ++t) n += grp(t, nq);
    return n;
}
template <int NQ> __device__ __forceinline__ void wait_stage(int s) {
    if (s == 1) wait_vm<younger(1, NQ)>(); else if (s == 2) wait_vm<younger(2, NQ)>();
    else if (s == 3) wait_vm<younger(3, NQ)>(); else if (s == 4) wait_vm<younger(4, NQ)>();
    else if (s == 5) wait_vm<younger(5, NQ)>(); else if (s == 6) wait_vm<younger(6, NQ)>();
    else if (s == 7) wait_vm<younger(7, NQ)>(); else wait_vm<younger(8, NQ)>();
}
__device__ __forceinline__ void flag_store(void* sbase, unsigned voff, u64 v, unsigned mask) {
    asm volatile("s_mov_b32 exec_lo, %3\n\ts_mov_b32 exec_hi, 0\n\tglobal_store_dwordx2 %0, %1, %2 sc1\n\ts_mov_b64 exec, -1" :: "v"(voff), "v"(v), "s"(sbase), "s"(mask) : "memory");
}
#define PIN(x) asm volatile("" : "+v"(x))

__device__ __forceinline__ f32x16 attn_fallback(const char* kf, const char* vf, bf16x8 qf, int lane) {
    f32x16 o = {};
    float m = -INFINITY;
    const bool isv = ((lane >> 4) & 1) == 0;
    const u32x4 ones = {0x3f803f80u, 0x3f803f80u, 0x3f803f80u, 0x3f803f80u};
    const unsigned voff = (lane >> 5) * 256 + (lane & 15) * 16;
#pragma unroll 1
    for (int t = 0; t < NTT; ++t) {
        bf16x8 kk, a0, a1;
        gld16_sc1(kk, kf + t * 1024, lane * 16);
        gld16_sc1(a0, vf + t * 1024, voff);
        gld16_sc1(a1, vf + t * 1024 + 512, voff);
        wait_vm1<0>(kk);
        wait_vm2<0>(a0, a1);
        u32x4 v0 = __builtin_bit_cast(u32x4, a0), v1 = __builtin_bit_cast(u32x4, a1);
        if (!isv) { v0 = ones; v1 = ones; }
        f32x16 z = {};
        f32x16 s = MFMA(kk, qf, z);
        float tm = s[0];
#pragma unroll
        for (int i = 1; i < 16; ++i) tm = fmaxf(tm, s[i]);
        tm = fmaxf(tm, __shfl_xor(tm, 32));
        const float mn = fmaxf(m, tm);
        const float al = EXP2(m - mn);
        m = mn;
#pragma unroll
        for (int i = 0; i < 16; ++i) { o[i] *= al; s[i] = EXP2(s[i] - mn); }
        o = MFMA(__builtin_bit_cast(bf16x8, v0), pack8(s, 0), o);
        o = MFMA(__builtin_bit_cast(bf16x8, v1), pack8(s, 8), o);
    }
    return o;
}

__global__ __launch_bounds__(512, 2) void fused(const u16* __restrict__ x, const u16* __restrict__ wqkv, const u16* __restrict__ wo,
                                                const u16* __restrict__ wg, const u16* __restrict__ wu, const u16* __restrict__ wd,
                                                const u16* __restrict__ gatew, char* __restrict__ ws, u16* __restrict__ out) {
    extern __shared__ __attribute__((aligned(16))) char smem[];
    unsigned mta = 0, mtb = 0;
    STAMP(0); STAMPC(61);
    const int tid = threadIdx.x, lane = tid & 63;
    const int wid = __builtin_amdgcn_readfirstlane(tid >> 6);
    const int xcd = blockIdx.x & 7, idx = blockIdx.x >> 3;
    const unsigned lds0 = 0;
    const unsigned loff = lane * 16;
    const int r = lane & 31, h = lane >> 5, q = (lane & 15) >> 2, p4 = lane & 3, blk = (lane >> 4) & 1;
    const int rb = (xcd & 1) * 8 + (idx & 7), cg = (xcd >> 1) * 4 + (idx >> 3);
    const int tt = wid & 3, ch = wid >> 2, hg = cg >> 1, odd = cg & 1;
    const int rot = ROT * ((idx & 7) + 2 * (idx >> 3));
    unsigned xoff[2], woff[2], foff[3];
#pragma unroll
    for (int e = 0; e < 2; ++e) {
        const int row = wid * 16 + e * 8 + (lane >> 3);
        xoff[e] = (unsigned)((rb * 128 + row) * 1024 + (((lane & 7) ^ ((row >> 1) & 7)) * 16));
    }
    const int wp0 = ch ? 2 * wid - 4 : wid;
#pragma unroll
    for (int e = 0; e < 2; ++e) {
        const int n = (wp0 + e) * 64 + lane, kr = n / 12, c = n % 12;
        woff[e] = (unsigned)(kr * 3072 + hg * 128 + (c < 8 ? odd * 2048 + c * 16 : 1024 - 128 + odd * 64 + c * 16));
    }
    unsigned ldx = lds0 + wid * 2048, ldw = lds0 + wp0 * 1024;
    SPIN(ldx); SPIN(ldw);
    auto issue_q = [&](int st, int op) {
        const int kb = (st + rot) & 7;
        if (op == 0) glds16((const char*)x + kb * 128, xoff[0], ldx, phys(st));
        if (op == 1) glds16((const char*)wqkv + kb * (64 * 3072), woff[0], ldw, phys(st) + 16384);
        if (op == 2) glds16((const char*)x + kb * 128, xoff[1], ldx, phys(st) + 1024);
        if (op == 3) glds16((const char*)wqkv + kb * (64 * 3072), woff[1], ldw, phys(st) + 16384 + 1024);
    };
    if (wid == 3) glds16(gatew, (loff + rot * 128) & 1023, lds0, 0);
#pragma unroll
    for (int st = 0; st < 5; ++st) {
        issue_q(st, 0); issue_q(st, 1); issue_q(st, 2);
        if (ch) issue_q(st, 3);
        if (st < IBAR) asm volatile("s_barrier" ::: "memory");
    }
    STAMP(1);
    const u64 ep = *(const __attribute__((address_space(4))) u64*)(uintptr_t)(ws + WS_EP);
    const u64 tag = (ep ^ (ep >> 31)) * 0x9E3779B97F4A7C15ull + 0xD1B54A32D192ED03ull;
    unsigned lead = __builtin_amdgcn_readfirstlane(tid < 64 ? 1 : 0);
    SPIN(lead);
    const int u0 = cg < 14 ? cg * 6 : 80;
#pragma unroll
    for (int e = 0; e < 3; ++e) {
        const int n = (tt * 3 + e) * 64 + lane, kr = n / 12, c = n % 12;
        foff[e] = (unsigned)(kr * (F_ * 2) + u0 * 32 + c * 16);
    }
    const char* fsrc = (const char*)(ch ? wu : wg);
    unsigned fdst = lds0 + F1OFF + ch * F1UP + tt * 3072;
    SPIN(fdst);
    auto issue_f = [&](int st, int op) {
        if (op < 3) glds16(fsrc + ((st + rot) & 7) * (64 * F_ * 2), foff[op], fdst, phys(st) + op * 1024);
    };
    bf16x8 xr[32];
    const int T = rb * 4 + tt;
    bf16x8 fw[3][3];
    ldsptr frd = (ldsptr)smem + F1OFF + (8 * h + q) * 192 + blk * F1UP + p4 * 8 + ch * 96, fs_;
    auto frd_ = [&](int g) {
        if ((g & 3) == 0) fs_ = ladd(frd, phys(8 + (g >> 2)));
        const int so = (g & 3) * 3072;
#pragma unroll
        for (int u = 0; u < 3; ++u) fw[g % 3][u] = tr_frag(fs_ + so + u * 32, fs_ + so + u * 32 + 768);
    };
    {
        ldsptr xrd[4];
#pragma unroll
        for (int ks = 0; ks < 4; ++ks) xrd[ks] = (ldsptr)smem + (tt * 32 + r) * 128 + (((2 * ks + h) ^ ((r >> 1) & 7)) * 16);
        ldsptr wrd = (ldsptr)smem + 16384 + (8 * h + q) * 192 + blk * 32 + p4 * 8 + ch * 128;
        ldsptr grd = (ldsptr)smem + h * 16;
        auto qkv = [&](auto var) {
            constexpr int VAR = decltype(var)::value;
            constexpr int NQ = VAR == 2 ? 4 : 3;
            f32x16 a0 = {}, a1 = {};
            float gacc = 0.f;
            bf16x8 w0[3], w1[3];
            ldsptr ws_;
            auto rd = [&](int g) {
                const int so = phys(g >> 2), ks = g & 3;
                if (ks == 0) ws_ = ladd(wrd, so);
                xr[g] = lds_b128(ladd(xrd[ks], so));
                w0[g % 3] = tr_frag(ws_ + ks * 3072, ws_ + ks * 3072 + 768);
                if (VAR != 2) w1[g % 3] = tr_frag(ws_ + ks * 3072 + 64, ws_ + ks * 3072 + 64 + 768);
                else w1[g % 3] = lds_b128(grd + g * 32);
            };
            wait_vm<younger(0, NQ)>();
            bar();
            STAMP(2);
            rd(0);
            rd(1);
#pragma unroll
            for (int g = 0; g < 32; ++g) {
                const int s = (g + 2) >> 2, op = (g + 2) & 3;
                if (op == 0) {
                    wait_stage<NQ>(s);
                    bar();
                    STAMP(2 + s);
                }
                if (g < 30) rd(g + 2); else frd_(g - 30);
                SBAR();
                if (VAR == 1) { a0 = MFMA(xr[g], w0[g % 3], a0); a1 = MFMA(xr[g], w1[g % 3], a1); }
                else { a0 = MFMA(w0[g % 3], xr[g], a0); if (VAR == 0) a1 = MFMA(w1[g % 3], xr[g], a1); }
                PIN(a0); if (VAR != 2) PIN(a1);
                SBAR();
                if (s >= 1 && !NODMA) { if (s + 4 <= 7) { if (op < NQ) issue_q(s + 4, op); } else issue_f(s + 4, op); }
                if (VAR == 2 && !NOGATE) {
#pragma unroll
                    for (int j = 0; j < 4; ++j) {
                        const bf16x2 xa = {xr[g][2 * j], xr[g][2 * j + 1]}, ga = {w1[g % 3][2 * j], w1[g % 3][2 * j + 1]};
                        gacc = __builtin_amdgcn_fdot2_f32_bf16(xa, ga, gacc, false);
                    }
                    PIN(gacc);
                }
                SBAR();
            }
            STAMP(11);
            if (VAR == 0) {
                char* qp = ws + WS_Q + ((size_t)(4 * hg) * NTT + T) * 1024;
                gst16_sc1(qp, loff, pack8(a0, 0, QSCALE));
                gst16_sc1(qp + NTT * 1024, loff, pack8(a0, 8, QSCALE));
                gst16_sc1(qp + 2 * NTT * 1024, loff, pack8(a1, 0, QSCALE));
                gst16_sc1(qp + 3 * NTT * 1024, loff, pack8(a1, 8, QSCALE));
            } else if (VAR == 1) {
                char* vp = ws + WS_V + ((size_t)(4 * hg) * NTT + T) * 1024;
                const unsigned vo = blk * (NTT * 1024) + h * 256 + (lane & 15) * 16;
                gst16_sc1(vp, vo, pack8(a0, 0));
                gst16_sc1(vp + 512, vo, pack8(a0, 8));
                gst16_sc1(vp + 2 * NTT * 1024, vo, pack8(a1, 0));
                gst16_sc1(vp + 2 * NTT * 1024 + 512, vo, pack8(a1, 8));
            } else {
                char* kp = ws + WS_K + ((size_t)(4 * hg + 2 * odd) * NTT + T) * 1024;
                gst16_sc1(kp, loff, pack8(a0, 0));
                gst16_sc1(kp + NTT * 1024, loff, pack8(a0, 8));
                gacc += __shfl_xor(gacc, 32);
                gst4_sc1(ws + WS_G + (cg * S_ + T * 32) * 4, r * 4, __builtin_amdgcn_rcpf(1.0f + EXP2(-LOG2E * gacc)));
            }
        };
        if (ch) qkv(std::integral_constant<int, 2>{});
        else if (odd) qkv(std::integral_constant<int, 1>{});
        else qkv(std::integral_constant<int, 0>{});
    }
    const int head = xcd * 4 + (idx & 3), qb = idx >> 2;
    const int AT = qb * 8 + wid;
    const int krot = KROT * 8 * qb;
    const char* kf = ws + WS_K + (size_t)head * NTT * 1024;
    const char* vf = ws + WS_V + (size_t)head * NTT * 1024;
    const bool isv = blk == 0;
    const unsigned voff = h * 256 + (lane & 15) * 16;
    const u32x4 ones = {0x3f803f80u, 0x3f803f80u, 0x3f803f80u, 0x3f803f80u};
    const int mb = xcd * 4 + (idx & 3), nb = idx >> 2;
    const int mh = wid & 1, kq = wid >> 1;
    bf16x8 qf, k0, kk, g0, g1;
    float agate, tgate;
    unsigned ldv = lds0 + wid * 1040, ldk = lds0 + wid * 1024;
    SPIN(ldv); SPIN(ldk);
    auto issue_kv = [&](int c, int op) {
        if (op == 0) glds16_sc1(vf + ((8 * c + 1 + wid + krot) & 63) * 1024, loff, ldv, c * CHK);
        else glds16_sc1(kf + ((8 * c + 2 + wid + krot) & 63) * 1024, loff, ldk, c * CHK + KOFF);
    };
    f32x16 o, acc[4];
    bf16x8 ha[3], hb[3];
    {
        f32x16 fa[3] = {};
        u64 pollv = 0;
        const char* fq = ws + WS_FQ + xcd * 256;
        STAMP(12);
#pragma unroll
        for (int g = 0; g < 32; ++g) {
            const int s = 8 + ((g + 2) >> 2), op = (g + 2) & 3;
            if (op == 0 && s <= 14) {
                if (s == 9) wait_vm<12>();
                else if (s == 10) wait_vm<4>();
                else if (s == 12) wait_vm<9>();
                else if (s == 13) wait_vm<6>();
                else if (s == 14) {
                    asm volatile("s_waitcnt vmcnt(0)" : "+v"(pollv) :: "memory");
                    if (wid == 0 && !__all(pollv == tag)) spin_flags(fq + r * 8, true, tag);
                }
                bar();
                STAMP(4 + s);
                if (s == 10) flag_store(ws + WS_FQ + hg * 256 + (odd * 16 + rb) * 8, 0, tag, lead);
            }
            if (g < 30) frd_(g + 2);
            SBAR();
#pragma unroll
            for (int u = 0; u < 3; ++u) fa[u] = MFMA(fw[g % 3][u], xr[g], fa[u]);
            PIN(fa[0]); PIN(fa[1]); PIN(fa[2]);
            SBAR();
            if (s <= 11 && !NODMA) issue_f(s + 4, op);
            if (g == POLLG) asm volatile("global_load_dwordx2 %0, %1, %2 sc1" : "+v"(pollv) : "v"(r * 8), "s"(fq) : "memory");
            if (g == 22) { gld16_sc1(qf, ws + WS_Q + ((size_t)head * NTT + AT) * 1024, loff); gld16_sc1(k0, kf + krot * 1024, loff); }
            if (g == 23) { gld16_sc1(g0, vf + krot * 1024, voff); gld16_sc1(g1, vf + krot * 1024 + 512, voff); }
            if (g == 24) { gld16_sc1(kk, kf + (krot + 1) * 1024, loff); issue_kv(0, 0); }
            if (g == 25) { issue_kv(0, 1); gld4_sc1(agate, ws + WS_G + (2 * xcd * S_ + AT * 32) * 4, r * 4); }
            if (g >= 26) issue_kv(1 + (g - 26) / 2, g & 1);
            if (g == 26) gld4_sc1(tgate, ws + WS_G + (2 * xcd * S_ + (2 * mb + mh) * 32) * 4, r * 4);
            if (g == 22) *(LDSP u32x4*)((ldsptr)smem + min(wid, 5) * CHK + (lane & 7) * 1040 + 1024) = ones;
            if (g >= 24 && g < 28) { acc[g - 24] = f32x16{}; PIN(acc[g - 24]); }
            if (g == 28) { o = f32x16{}; PIN(o); }
            if (g >= 29) { ha[g - 29] = bf16x8{}; hb[g - 29] = bf16x8{}; PIN(ha[g - 29]); PIN(hb[g - 29]); }
            SBAR();
        }
        STAMP(20);
        char* hp = ws + WS_H + ((size_t)T * NKF + u0 + ch * 3) * 1024;
#pragma unroll
        for (int u = 0; u < 3; ++u) {
            f32x16 t;
#pragma unroll
            for (int j = 0; j < 8; ++j) {
                const float g = fa[u][j];
                t[j] = g * fa[u][8 + j] * __builtin_amdgcn_rcpf(1.0f + EXP2(-LOG2E * g));
            }
            const bool own = cg < 14 || (cg == 14 && ch * 3 + u >= 4);
            gst16_sc1(own ? hp + u * 1024 : ws + WS_DUMP + (blockIdx.x * 8 + wid) * 1024, loff, pack8(t, 0));
        }
    }
    const int fk0 = wid * 11 - (wid > 6 ? 1 : 0) - (wid < 6 ? 1 : 2);
    unsigned doff;
    {
        const int row = lane >> 3;
        doff = (unsigned)(row * 1024 + nb * 128 + (((lane & 7) ^ (((row >> 1) & 1) << 2)) * 16));
    }
    const int row0 = 4 * h + q, sw = (row0 >> 1) & 1;
    const int wfo0 = row0 * 128 + sw * 64 + (p4 & 1) * 32 + blk * 16 + (p4 >> 1) * 8, wfo1 = wfo0 ^ 64;
    const char* hfa = ws + WS_H + ((ptrdiff_t)(2 * mb + mh) * NKF + fk0) * 1024;
    const char* hfb = ws + WS_H + ((ptrdiff_t)(2 * mb + 1 - mh) * NKF + fk0) * 1024;
    const char* wdp = (const char*)wd + (ptrdiff_t)fk0 * 16384;
    {
        asm volatile("s_waitcnt vmcnt(11)" : "+v"(qf), "+v"(k0), "+v"(kk), "+v"(g0), "+v"(g1) :: "memory");
        STAMP(21);
        f32x16 z = {};
        f32x16 s = MFMA(k0, qf, z);
        bar();
        float m = fmaxf(s[0], s[1]);
#pragma unroll
        for (int i = 2; i < 16; i += 2) m = fmaxf(fmaxf(m, s[i]), s[i + 1]);
        m = fmaxf(m, __shfl_xor(m, 32));
        f32x16 negm;
#pragma unroll
        for (int i = 0; i < 16; ++i) negm[i] = -m;
        s = MFMA(k0, qf, negm);
        bf16x8 v0 = isv ? g0 : __builtin_bit_cast(bf16x8, ones), v1 = isv ? g1 : __builtin_bit_cast(bf16x8, ones);
        u32x4 p1 = {0, 0, 0, 0};
        bf16x8 v1p = v1;
        ldsptr krd = (ldsptr)smem + KOFF + lane * 16;
        ldsptr vrd0 = (ldsptr)smem + (isv ? voff : 1024);
        ldsptr vrd1 = (ldsptr)smem + (isv ? 512 + voff : 1024);
        ldsptr wr0 = (ldsptr)smem + wid * RING + wfo0, wr1 = (ldsptr)smem + wid * RING + wfo1;
        unsigned ringb = lds0 + wid * RING;
        SPIN(ringb);
        auto refill = [&](int e, const char* wsrc, int n, u64 mask) {
            glds16(wsrc, doff, ringb, e * 2048);
            glds16(wsrc + 8192, doff, ringb, e * 2048 + 1024);
            asm volatile("s_mov_b64 exec, %5\n\tglobal_load_dwordx4 %0, %2, %3 sc1\n\tglobal_load_dwordx4 %1, %2, %4 sc1\n\ts_mov_b64 exec, -1"
                         : "+v"(ha[e]), "+v"(hb[e]) : "v"(loff), "s"(hfa + n * 1024), "s"(hfb + n * 1024), "s"(mask) : "memory");
        };
        auto chunk = [&](auto fused_, const char* ws0, int wst0, int wst, int n0, u64 m0_, u64 m1_, u64 m2_) {
            constexpr bool FU = decltype(fused_)::value;
            bf16x8 wf;
#pragma unroll
            for (int i = 0; i < 8; ++i) {
                const int e = i / 3, ph = i % 3;
                SBAR();
                f32x16 sn = MFMA(kk, qf, negm);
                SBAR();
                kk = lds_b128(krd + i * 1024);
                bf16x8 nv0 = lds_b128(vrd0 + i * 1040);
                if (FU && ph == 0) wait_vm2<8>(ha[e], hb[e]);
                if (FU && ph < 2) wf = tr_frag((ph ? wr1 : wr0) + e * 2048, (ph ? wr1 : wr0) + e * 2048 + 1024);
                SBAR();
                u32x4 p0;
                { float a = EXP2(s[0]), b2 = EXP2(s[1]), c2 = EXP2(s[2]), d = EXP2(s[3]); p0[0] = cvtpk(a, b2); p0[1] = cvtpk(c2, d); }
                SBAR();
                o = MFMA(v1p, __builtin_bit_cast(bf16x8, p1), o);
                SBAR();
                v1p = v1;
                v1 = lds_b128(vrd1 + i * 1040);
                SBAR();
                { float a = EXP2(s[4]), b2 = EXP2(s[5]), c2 = EXP2(s[6]), d = EXP2(s[7]); p0[2] = cvtpk(a, b2); p0[3] = cvtpk(c2, d); }
                SBAR();
                float e8 = EXP2(s[8]), e9 = EXP2(s[9]);
                SBAR();
                o = MFMA(v0, __builtin_bit_cast(bf16x8, p0), o);
                SBAR();
                v0 = nv0;
                { float c2 = EXP2(s[10]), d = EXP2(s[11]); p1[0] = cvtpk(e8, e9); p1[1] = cvtpk(c2, d); }
                SBAR();
                if (FU && ph < 2) acc[ph] = MFMA(wf, ha[e], acc[ph]);
                SBAR();
                float e12 = EXP2(s[12]), e13 = EXP2(s[13]), e14 = EXP2(s[14]), e15 = EXP2(s[15]);
                SBAR();
                if (FU && ph < 2) acc[2 + ph] = MFMA(wf, hb[e], acc[2 + ph]);
                SBAR();
                p1[2] = cvtpk(e12, e13); p1[3] = cvtpk(e14, e15);
                SBAR();
                if (FU && (ph == 2 || i == 7)) refill(e, ws0 + (e == 0 ? wst0 : e == 1 ? 16384 : wst), n0 + e, e == 0 ? m0_ : e == 1 ? m1_ : m2_);
                SBAR();
                s = sn;
            }
            krd += CHK; vrd0 += CHK; vrd1 += CHK;
        };
        STAMP(22);
#pragma unroll 1
        for (int c = 0; c < 3; ++c) {
            if (c == 0) {
                issue_kv(4, 0); issue_kv(4, 1); issue_kv(5, 0); issue_kv(5, 1);
                if (wid == 7 && lane >= 48) *(LDSP u32x4*)((ldsptr)smem + (lane >> 3) * CHK + (lane & 7) * 1040 + 1024) = ones;
            } else if (c == 1) {
                wait_vm<4>();
                bar();
                flag_store(ws + WS_FH + rb * 128 + cg * 8, 0, tag, lead);
                issue_kv(6, 0); issue_kv(6, 1);
                if (wid < 7) issue_kv(7, 0);
                if (wid < 6) issue_kv(7, 1);
            } else {
#pragma unroll
                for (int e = 0; e < 4; ++e) glds16((const char*)wo + (wid * 4 + e) * 8192, doff, ldk * 4, WO_LO + e * 1024);
                if (wid == 0) glds4_sc1(ws + WS_FH + (mb >> 1) * 128, r * 4, lds0, POLL);
            }
            STAMP(30 + c);
            chunk(std::false_type{}, nullptr, 0, 0, 0, 0, 0, 0);
        }
        wait_vm<0>();
        if (wid == 0) {
            const u64 v = *(LDSP u64*)((ldsptr)smem + POLL + (lane & 15) * 8);
            if (!__all((lane & 15) == 15 || v == tag)) spin_flags(ws + WS_FH + (mb >> 1) * 128 + (lane & 15) * 8, (lane & 15) != 15, tag);
        }
        bar();
#pragma unroll 1
        for (int c = 3; c < 8; ++c) {
            STAMP(30 + c);
            const int n0 = c < 7 ? 3 * c - 9 : 2;
            const char* w0 = c < 7 ? wdp + n0 * 16384 : (const char*)wo + (16 + 2 * wid) * 16384;
            const u64 ml = c < 7 ? ~0ull : 1ull;
            chunk(std::true_type{}, w0, c == 3 ? 16384 : 0, c < 7 ? 32768 : 0, n0, c == 3 ? 0ull : ml, c == 3 && wid >= 6 ? 0ull : ml, ml);
        }
        o = MFMA(v1p, __builtin_bit_cast(bf16x8, p1), o);
        asm volatile("s_waitcnt vmcnt(0)" : "+v"(ha[0]), "+v"(hb[0]), "+v"(ha[1]), "+v"(hb[1]), "+v"(ha[2]), "+v"(hb[2]), "+v"(agate), "+v"(tgate) :: "memory");
    }
    STAMP(40);
    {
        float chk = 0.f;
#pragma unroll
        for (int i = 0; i < 8; ++i) chk += fabsf(o[i]) * 0.125f;
        const bool bad = !(o[8] <= 1.8446744e19f) || !(chk <= 3.0e38f);
        if (__builtin_expect(__any(bad), 0)) o = attn_fallback(kf, vf, qf, lane);
        const float rl = agate * __builtin_amdgcn_rcpf(o[8]);
        gst16_sc1(ws + WS_O + ((size_t)AT * NH + head) * 1024, loff, pack8(o, 0, rl));
        wait_vm<0>();
        bar();
        if (tid < 8) __hip_atomic_store((gu64*)(ws + WS_FO + tid * 2048 + qb * 256) + head, tag, RLX_AGENT);
    }
    STAMP(41);
    LDSP f32x4* xch = (LDSP f32x4*)((ldsptr)smem + XCH);
#pragma unroll
    for (int t = 0; t < 2; ++t)
#pragma unroll
        for (int i = 0; i < 4; ++i) {
            f32x4 v = {acc[2 + t][4 * i], acc[2 + t][4 * i + 1], acc[2 + t][4 * i + 2], acc[2 + t][4 * i + 3]};
            xch[wid * 512 + (t * 4 + i) * 64 + lane] = v;
        }
    bf16x8 wa[8][2];
    {
        const int base = kq < 2 ? WO_LO + kq * 16384 : (kq - 2) * 4 * RING, st2 = kq < 2 ? 4096 : RING;
#pragma unroll
        for (int j = 0; j < 8; ++j) {
            ldsptr p = (ldsptr)smem + base + (j >> 1) * st2 + (j & 1) * 2048;
            wa[j][0] = tr_frag(p + wfo0, p + wfo0 + 1024);
            wa[j][1] = tr_frag(p + wfo1, p + wfo1 + 1024);
        }
    }
    if (wid == 0) spin_flags(ws + WS_FO + nb * 2048 + (mb >> 2) * 256 + r * 8, true, tag);
    if (wid == 1 && blockIdx.x == 0) {
#pragma unroll
        for (int i = 0; i < 4; ++i) spin_flags(ws + WS_FQ + i * 512 + lane * 8, true, tag);
        if (lane == 0) __hip_atomic_store((gu64*)(ws + WS_EP), tag, RLX_AGENT);
    }
    bar();
    STAMP(42);
    const char* of = ws + WS_O + ((size_t)(2 * mb + mh) * NH + kq * 8) * 1024;
    bf16x8 oa[8];
    const float og = 1.0f - tgate;
#pragma unroll
    for (int j = 0; j < 8; ++j) gld16_sc1(oa[j], of + j * 1024, loff);
#pragma unroll
    for (int t = 0; t < 2; ++t)
#pragma unroll
        for (int i = 0; i < 4; ++i) {
            const f32x4 v = xch[(wid ^ 1) * 512 + (t * 4 + i) * 64 + lane];
#pragma unroll
            for (int j = 0; j < 4; ++j) acc[t][4 * i + j] = (acc[t][4 * i + j] + v[j]) * og;
        }
#pragma unroll
    for (int j = 0; j < 8; ++j) {
        if (j == 0) wait_vm1<7>(oa[j]); else if (j == 1) wait_vm1<6>(oa[j]); else if (j == 2) wait_vm1<5>(oa[j]);
        else if (j == 3) wait_vm1<4>(oa[j]); else if (j == 4) wait_vm1<3>(oa[j]); else if (j == 5) wait_vm1<2>(oa[j]);
        else if (j == 6) wait_vm1<1>(oa[j]); else wait_vm1<0>(oa[j]);
        acc[0] = MFMA(wa[j][0], oa[j], acc[0]);
        acc[1] = MFMA(wa[j][1], oa[j], acc[1]);
        SBAR();
    }
    STAMP(43);
#pragma unroll
    for (int t = 0; t < 2; ++t)
#pragma unroll
        for (int i = 0; i < 4; ++i) {
            f32x4 v = {acc[t][4 * i], acc[t][4 * i + 1], acc[t][4 * i + 2], acc[t][4 * i + 3]};
            xch[(wid ^ 1) * 512 + (t * 4 + i) * 64 + lane] = v;
        }
    bar();
    f32x4 ra = {}, rb4 = {};
#pragma unroll
    for (int k = 0; k < 4; ++k) {
        ra += xch[((2 * k + mh) ^ 1) * 512 + (kq * 2) * 64 + lane];
        rb4 += xch[((2 * k + mh) ^ 1) * 512 + (kq * 2 + 1) * 64 + lane];
    }
    const u32x4 ov = {cvtpk(ra[0], ra[1]), cvtpk(ra[2], ra[3]), cvtpk(rb4[0], rb4[1]), cvtpk(rb4[2], rb4[3])};
    *(u32x4*)((char*)out + (size_t)(mb * 64 + mh * 32 + r) * 1024 + (nb * 64 + (kq >> 1) * 32 + h * 16 + (kq & 1) * 8) * 2) = ov;
    STAMP(50);
    if (MA < 60) { const unsigned te = (unsigned)rt() + MK * (mtb - mta); while ((int)((unsigned)rt() - te) < 0) __builtin_amdgcn_s_sleep(1); }
    else { const unsigned te = (unsigned)ct() + 40000; while ((int)((unsigned)ct() - te) < 0) __builtin_amdgcn_s_sleep(1); }
}

extern "C" void kernel_launch(void* const* d_in, const int* in_sizes, int n_in, void* d_out, int out_size, void* d_ws,
                              size_t ws_size, hipStream_t stream) {
    (void)hipFuncSetAttribute((const void*)fused, hipFuncAttributeMaxDynamicSharedMemorySize, LDS_BYTES);
    fused<<<256, 512, LDS_BYTES, stream>>>((const u16*)d_in[0], (const u16*)d_in[1], (const u16*)d_in[2], (const u16*)d_in[4],
                                           (const u16*)d_in[5], (const u16*)d_in[6], (const u16*)d_in[7], (char*)d_ws, (u16*)d_out);
}
```

```cpp
#define OUTM 3
#include <hip/hip_runtime.h>
#include <hip/hip_bf16.h>
#include <stdint.h>
#include <type_traits>

typedef unsigned short u16;
typedef unsigned long long u64;
typedef __attribute__((ext_vector_type(8))) __bf16 bf16x8;
typedef __attribute__((ext_vector_type(2))) __bf16 bf16x2;
typedef __attribute__((ext_vector_type(4))) short s16x4;
typedef __attribute__((ext_vector_type(16))) float f32x16;
typedef __attribute__((ext_vector_type(4))) float f32x4;
typedef __attribute__((ext_vector_type(2))) float f32x2;
typedef __attribute__((ext_vector_type(4))) unsigned u32x4;
#define LDSP __attribute__((address_space(3)))
typedef LDSP char* ldsptr;
typedef __attribute__((address_space(1))) unsigned gu32;
typedef __attribute__((address_space(1))) u64 gu64;

constexpr int S_ = 2048, F_ = 1376, NH = 32;
constexpr int NTT = S_ / 32;
constexpr int NKF = F_ / 16;
constexpr float LOG2E = 1.4426950408889634f;
constexpr float QSCALE = 0.25f * LOG2E;

constexpr size_t WS_EP = 0;
constexpr size_t WS_FQ = 1024;
constexpr size_t WS_FH = WS_FQ + 2048;
constexpr size_t WS_FO = WS_FH + 2048;
constexpr size_t WS_G = WS_FO + 16384;
constexpr size_t WS_Q = 262144;
constexpr size_t WS_K = WS_Q + (size_t)NH * NTT * 1024;
constexpr size_t WS_V = WS_K + (size_t)NH * NTT * 1024;
constexpr size_t WS_O = WS_V + (size_t)NH * NTT * 1024;
constexpr size_t WS_H = WS_O + (size_t)NTT * 32 * 1024;

constexpr size_t WS_DUMP = WS_H + (size_t)NTT * NKF * 1024;

constexpr int LDS_BYTES = 163840;
constexpr int SLOT = 28672;
__host__ __device__ constexpr int phys(int st) { return 20480 + ((st + 4) % 5) * SLOT; }
constexpr int F1OFF = 4064, F1UP = 12320;
constexpr int CHK = 16512, KOFF = 8320;
constexpr int WO_LO = 130048, POLL = 162816, XCH = 49536, RING = 6144;

#define MFMA(a, b, c) __builtin_amdgcn_mfma_f32_32x32x16_bf16((a), (b), (c), 0, 0, 0)
#define NODMA 0
#define NOGATE 0
#define ROT 1
#define KROT 1
#define IBAR 4
#define POLLG 18
#define SPIN(x) asm volatile("" : "+s"(x))
#define SBAR() __builtin_amdgcn_sched_barrier(0)
#define EXP2(x) __builtin_amdgcn_exp2f(x)
#define RLX_AGENT __ATOMIC_RELAXED, __HIP_MEMORY_SCOPE_AGENT
#define STAMP(i)
#define STAMPC(i)

__device__ __forceinline__ unsigned cvtpk(float lo, float hi) {
    f32x2 v = {lo, hi};
    return __builtin_bit_cast(unsigned, __builtin_convertvector(v, bf16x2));
}
__device__ __forceinline__ void glds16(const void* sbase, unsigned voff, unsigned lds_s, int lds_i) {
    asm volatile("s_add_u32 m0, %2, %3\n\ts_nop 0\n\tglobal_load_lds_dwordx4 %0, %1" :: "v"(voff), "s"(sbase), "s"(lds_s), "i"(lds_i) : "memory", "scc");
}
__device__ __forceinline__ void glds16_sc1(const void* sbase, unsigned voff, unsigned lds_s, int lds_i) {
    asm volatile("s_add_u32 m0, %2, %3\n\ts_nop 0\n\tglobal_load_lds_dwordx4 %0, %1 sc1" :: "v"(voff), "s"(sbase), "s"(lds_s), "i"(lds_i) : "memory", "scc");
}
__device__ __forceinline__ void glds4_sc1(const void* sbase, unsigned voff, unsigned lds_s, int lds_i) {
    asm volatile("s_add_u32 m0, %2, %3\n\ts_nop 0\n\tglobal_load_lds_dword %0, %1 sc1" :: "v"(voff), "s"(sbase), "s"(lds_s), "i"(lds_i) : "memory", "scc");
}
__device__ __forceinline__ void gld16_sc1(bf16x8& r, const void* sbase, unsigned voff) {
    asm volatile("global_load_dwordx4 %0, %1, %2 sc1" : "=v"(r) : "v"(voff), "s"(sbase) : "memory");
}
__device__ __forceinline__ void gld16_sc1_tied(bf16x8& r, const void* sbase, unsigned voff) {
    asm volatile("global_load_dwordx4 %0, %1, %2 sc1" : "+v"(r) : "v"(voff), "s"(sbase) : "memory");
}
__device__ __forceinline__ void gld4_sc1(float& r, const void* sbase, unsigned voff) {
    asm volatile("global_load_dword %0, %1, %2 sc1" : "=v"(r) : "v"(voff), "s"(sbase) : "memory");
}
__device__ __forceinline__ void gst16_sc1(void* sbase, unsigned voff, bf16x8 v) {
    asm volatile("global_store_dwordx4 %0, %1, %2 sc1\n\ts_nop 1" :: "v"(voff), "v"(v), "s"(sbase) : "memory");
}
__device__ __forceinline__ void gst4_sc1(void* sbase, unsigned voff, float v) {
    asm volatile("global_store_dword %0, %1, %2 sc1" :: "v"(voff), "v"(v), "s"(sbase) : "memory");
}
template <int N> __device__ __forceinline__ void wait_vm() { asm volatile("s_waitcnt vmcnt(%0)" ::"i"(N) : "memory"); }
template <int N> __device__ __forceinline__ void wait_vm1(bf16x8& a) { asm volatile("s_waitcnt vmcnt(%1)" : "+v"(a) : "i"(N) : "memory"); }
template <int N> __device__ __forceinline__ void wait_vm2(bf16x8& a, bf16x8& b) {
    asm volatile("s_waitcnt vmcnt(%2)" : "+v"(a), "+v"(b) : "i"(N) : "memory");
}
__device__ __forceinline__ void bar() { asm volatile("s_waitcnt lgkmcnt(0)\n\ts_barrier" ::: "memory"); }
__device__ __forceinline__ ldsptr ladd(ldsptr p, int off) {
    unsigned r;
    asm volatile("v_add_u32 %0, %1, %2" : "=v"(r) : "i"(off), "v"((unsigned)(uintptr_t)p));
    return (ldsptr)(uintptr_t)r;
}
__device__ __forceinline__ bf16x8 lds_b128(ldsptr p) { return *(LDSP bf16x8*)p; }
__device__ __forceinline__ bf16x8 tr_frag(ldsptr p0, ldsptr p1) {
    s16x4 a = __builtin_amdgcn_ds_read_tr16_b64_v4i16((LDSP s16x4*)p0), b = __builtin_amdgcn_ds_read_tr16_b64_v4i16((LDSP s16x4*)p1);
    typedef __attribute__((ext_vector_type(8))) short s16x8;
    s16x8 v = {a[0], a[1], a[2], a[3], b[0], b[1], b[2], b[3]};
    return __builtin_bit_cast(bf16x8, v);
}
__device__ __forceinline__ bf16x8 pack8(const f32x16& a, int o, float sc) {
    u32x4 v = {cvtpk(a[o] * sc, a[o + 1] * sc), cvtpk(a[o + 2] * sc, a[o + 3] * sc),
               cvtpk(a[o + 4] * sc, a[o + 5] * sc), cvtpk(a[o + 6] * sc, a[o + 7] * sc)};
    return __builtin_bit_cast(bf16x8, v);
}
__device__ __forceinline__ bf16x8 pack8(const f32x16& a, int o) {
    u32x4 v = {cvtpk(a[o], a[o + 1]), cvtpk(a[o + 2], a[o + 3]), cvtpk(a[o + 4], a[o + 5]), cvtpk(a[o + 6], a[o + 7])};
    return __builtin_bit_cast(bf16x8, v);
}
__device__ __forceinline__ void spin_flags(const char* p, bool need, u64 tag) {
#pragma unroll 1
    for (int spin = 0; spin < (1 << 20); ++spin) {
        const u64 v = __hip_atomic_load((gu64*)p, RLX_AGENT);
        if (__all(!need || v == tag)) break;
        __builtin_amdgcn_s_sleep(2);
    }
}

__host__ __device__ constexpr int grp(int s, int nq) { return s <= 7 ? nq : 3; }
__host__ __device__ constexpr int younger(int s, int nq) {
    int n = 0;
    for (int t = s + 1; t <= (s == 0 ? 4 : s + 3); ++t) n += grp(t, nq);
    return n;
}
template <int NQ> __device__ __forceinline__ void wait_stage(int s) {
    if (s == 1) wait_vm<younger(1, NQ)>(); else if (s == 2) wait_vm<younger(2, NQ)>();
    else if (s == 3) wait_vm<younger(3, NQ)>(); else if (s == 4) wait_vm<younger(4, NQ)>();
    else if (s == 5) wait_vm<younger(5, NQ)>(); else if (s == 6) wait_vm<younger(6, NQ)>();
    else if (s == 7) wait_vm<younger(7, NQ)>(); else wait_vm<younger(8, NQ)>();
}
__device__ __forceinline__ void flag_store(void* sbase, unsigned voff, u64 v, unsigned mask) {
    asm volatile("s_mov_b32 exec_lo, %3\n\ts_mov_b32 exec_hi, 0\n\tglobal_store_dwordx2 %0, %1, %2 sc1\n\ts_mov_b64 exec, -1" :: "v"(voff), "v"(v), "s"(sbase), "s"(mask) : "memory");
}
#define PIN(x) asm volatile("" : "+v"(x))

__device__ __forceinline__ f32x16 attn_fallback(const char* kf, const char* vf, bf16x8 qf, int lane) {
    f32x16 o = {};
    float m = -INFINITY;
    const bool isv = ((lane >> 4) & 1) == 0;
    const u32x4 ones = {0x3f803f80u, 0x3f803f80u, 0x3f803f80u, 0x3f803f80u};
    const unsigned voff = (lane >> 5) * 256 + (lane & 15) * 16;
#pragma unroll 1
    for (int t = 0; t < NTT; ++t) {
        bf16x8 kk, a0, a1;
        gld16_sc1(kk, kf + t * 1024, lane * 16);
        gld16_sc1(a0, vf + t * 1024, voff);
        gld16_sc1(a1, vf + t * 1024 + 512, voff);
        wait_vm1<0>(kk);
        wait_vm2<0>(a0, a1);
        u32x4 v0 = __builtin_bit_cast(u32x4, a0), v1 = __builtin_bit_cast(u32x4, a1);
        if (!isv) { v0 = ones; v1 = ones; }
        f32x16 z = {};
        f32x16 s = MFMA(kk, qf, z);
        float tm = s[0];
#pragma unroll
        for (int i = 1; i < 16; ++i) tm = fmaxf(tm, s[i]);
        tm = fmaxf(tm, __shfl_xor(tm, 32));
        const float mn = fmaxf(m, tm);
        const float al = EXP2(m - mn);
        m = mn;
#pragma unroll
        for (int i = 0; i < 16; ++i) { o[i] *= al; s[i] = EXP2(s[i] - mn); }
        o = MFMA(__builtin_bit_cast(bf16x8, v0), pack8(s, 0), o);
        o = MFMA(__builtin_bit_cast(bf16x8, v1), pack8(s, 8), o);
    }
    return o;
}

__global__ __launch_bounds__(512, 2) void fused(const u16* __restrict__ x, const u16* __restrict__ wqkv, const u16* __restrict__ wo,
                                                const u16* __restrict__ wg, const u16* __restrict__ wu, const u16* __restrict__ wd,
                                                const u16* __restrict__ gatew, char* __restrict__ ws, u16* __restrict__ out) {
    extern __shared__ __attribute__((aligned(16))) char smem[];
    STAMP(0); STAMPC(61);
    const int tid = threadIdx.x, lane = tid & 63;
    const int wid = __builtin_amdgcn_readfirstlane(tid >> 6);
    const int xcd = blockIdx.x & 7, idx = blockIdx.x >> 3;
    const unsigned lds0 = 0;
    const unsigned loff = lane * 16;
    const int r = lane & 31, h = lane >> 5, q = (lane & 15) >> 2, p4 = lane & 3, blk = (lane >> 4) & 1;
    const int rb = (xcd & 1) * 8 + (idx & 7), cg = (xcd >> 1) * 4 + (idx >> 3);
    const int tt = wid & 3, ch = wid >> 2, hg = cg >> 1, odd = cg & 1;
    const int rot = ROT * ((idx & 7) + 2 * (idx >> 3));
    unsigned xoff[2], woff[2], foff[3];
#pragma unroll
    for (int e = 0; e < 2; ++e) {
        const int row = wid * 16 + e * 8 + (lane >> 3);
        xoff[e] = (unsigned)((rb * 128 + row) * 1024 + (((lane & 7) ^ ((row >> 1) & 7)) * 16));
    }
    const int wp0 = ch ? 2 * wid - 4 : wid;
#pragma unroll
    for (int e = 0; e < 2; ++e) {
        const int n = (wp0 + e) * 64 + lane, kr = n / 12, c = n % 12;
        woff[e] = (unsigned)(kr * 3072 + hg * 128 + (c < 8 ? odd * 2048 + c * 16 : 1024 - 128 + odd * 64 + c * 16));
    }
    unsigned ldx = lds0 + wid * 2048, ldw = lds0 + wp0 * 1024;
    SPIN(ldx); SPIN(ldw);
    auto issue_q = [&](int st, int op) {
        const int kb = (st + rot) & 7;
        if (op == 0) glds16((const char*)x + kb * 128, xoff[0], ldx, phys(st));
        if (op == 1) glds16((const char*)wqkv + kb * (64 * 3072), woff[0], ldw, phys(st) + 16384);
        if (op == 2) glds16((const char*)x + kb * 128, xoff[1], ldx, phys(st) + 1024);
        if (op == 3) glds16((const char*)wqkv + kb * (64 * 3072), woff[1], ldw, phys(st) + 16384 + 1024);
    };
    if (wid == 3) glds16(gatew, (loff + rot * 128) & 1023, lds0, 0);
#pragma unroll
    for (int st = 0; st < 5; ++st) {
        issue_q(st, 0); issue_q(st, 1); issue_q(st, 2);
        if (ch) issue_q(st, 3);
        if (st < IBAR) asm volatile("s_barrier" ::: "memory");
    }
    STAMP(1);
    const u64 ep = *(const __attribute__((address_space(4))) u64*)(uintptr_t)(ws + WS_EP);
    const u64 tag = (ep ^ (ep >> 31)) * 0x9E3779B97F4A7C15ull + 0xD1B54A32D192ED03ull;
    unsigned lead = __builtin_amdgcn_readfirstlane(tid < 64 ? 1 : 0);
    SPIN(lead);
    const int u0 = cg < 14 ? cg * 6 : 80;
#pragma unroll
    for (int e = 0; e < 3; ++e) {
        const int n = (tt * 3 + e) * 64 + lane, kr = n / 12, c = n % 12;
        foff[e] = (unsigned)(kr * (F_ * 2) + u0 * 32 + c * 16);
    }
    const char* fsrc = (const char*)(ch ? wu : wg);
    unsigned fdst = lds0 + F1OFF + ch * F1UP + tt * 3072;
    SPIN(fdst);
    auto issue_f = [&](int st, int op) {
        if (op < 3) glds16(fsrc + ((st + rot) & 7) * (64 * F_ * 2), foff[op], fdst, phys(st) + op * 1024);
    };
    bf16x8 xr[32];
    const int T = rb * 4 + tt;
    bf16x8 fw[3][3];
    ldsptr frd = (ldsptr)smem + F1OFF + (8 * h + q) * 192 + blk * F1UP + p4 * 8 + ch * 96, fs_;
    auto frd_ = [&](int g) {
        if ((g & 3) == 0) fs_ = ladd(frd, phys(8 + (g >> 2)));
        const int so = (g & 3) * 3072;
#pragma unroll
        for (int u = 0; u < 3; ++u) fw[g % 3][u] = tr_frag(fs_ + so + u * 32, fs_ + so + u * 32 + 768);
    };
    {
        ldsptr xrd[4];
#pragma unroll
        for (int ks = 0; ks < 4; ++ks) xrd[ks] = (ldsptr)smem + (tt * 32 + r) * 128 + (((2 * ks + h) ^ ((r >> 1) & 7)) * 16);
        ldsptr wrd = (ldsptr)smem + 16384 + (8 * h + q) * 192 + blk * 32 + p4 * 8 + ch * 128;
        ldsptr grd = (ldsptr)smem + h * 16;
        auto qkv = [&](auto var) {
            constexpr int VAR = decltype(var)::value;
            constexpr int NQ = VAR == 2 ? 4 : 3;
            f32x16 a0 = {}, a1 = {};
            float gacc = 0.f;
            bf16x8 w0[3], w1[3];
            ldsptr ws_;
            auto rd = [&](int g) {
                const int so = phys(g >> 2), ks = g & 3;
                if (ks == 0) ws_ = ladd(wrd, so);
                xr[g] = lds_b128(ladd(xrd[ks], so));
                w0[g % 3] = tr_frag(ws_ + ks * 3072, ws_ + ks * 3072 + 768);
                if (VAR != 2) w1[g % 3] = tr_frag(ws_ + ks * 3072 + 64, ws_ + ks * 3072 + 64 + 768);
                else w1[g % 3] = lds_b128(grd + g * 32);
            };
            wait_vm<younger(0, NQ)>();
            bar();
            STAMP(2);
            rd(0);
            rd(1);
#pragma unroll
            for (int g = 0; g < 32; ++g) {
                const int s = (g + 2) >> 2, op = (g + 2) & 3;
                if (op == 0) {
                    wait_stage<NQ>(s);
                    bar();
                    STAMP(2 + s);
                }
                if (g < 30) rd(g + 2); else frd_(g - 30);
                SBAR();
                if (VAR == 1) { a0 = MFMA(xr[g], w0[g % 3], a0); a1 = MFMA(xr[g], w1[g % 3], a1); }
                else { a0 = MFMA(w0[g % 3], xr[g], a0); if (VAR == 0) a1 = MFMA(w1[g % 3], xr[g], a1); }
                PIN(a0); if (VAR != 2) PIN(a1);
                SBAR();
                if (s >= 1 && !NODMA) { if (s + 4 <= 7) { if (op < NQ) issue_q(s + 4, op); } else issue_f(s + 4, op); }
                if (VAR == 2 && !NOGATE) {
#pragma unroll
                    for (int j = 0; j < 4; ++j) {
                        const bf16x2 xa = {xr[g][2 * j], xr[g][2 * j + 1]}, ga = {w1[g % 3][2 * j], w1[g % 3][2 * j + 1]};
                        gacc = __builtin_amdgcn_fdot2_f32_bf16(xa, ga, gacc, false);
                    }
                    PIN(gacc);
                }
                SBAR();
            }
            STAMP(11);
            if (VAR == 0) {
                char* qp = ws + WS_Q + ((size_t)(4 * hg) * NTT + T) * 1024;
                gst16_sc1(qp, loff, pack8(a0, 0, QSCALE));
                gst16_sc1(qp + NTT * 1024, loff, pack8(a0, 8, QSCALE));
                gst16_sc1(qp + 2 * NTT * 1024, loff, pack8(a1, 0, QSCALE));
                gst16_sc1(qp + 3 * NTT * 1024, loff, pack8(a1, 8, QSCALE));
            } else if (VAR == 1) {
                char* vp = ws + WS_V + ((size_t)(4 * hg) * NTT + T) * 1024;
                const unsigned vo = blk * (NTT * 1024) + h * 256 + (lane & 15) * 16;
                gst16_sc1(vp, vo, pack8(a0, 0));
                gst16_sc1(vp + 512, vo, pack8(a0, 8));
                gst16_sc1(vp + 2 * NTT * 1024, vo, pack8(a1, 0));
                gst16_sc1(vp + 2 * NTT * 1024 + 512, vo, pack8(a1, 8));
            } else {
                char* kp = ws + WS_K + ((size_t)(4 * hg + 2 * odd) * NTT + T) * 1024;
                gst16_sc1(kp, loff, pack8(a0, 0));
                gst16_sc1(kp + NTT * 1024, loff, pack8(a0, 8));
                gacc += __shfl_xor(gacc, 32);
                gst4_sc1(ws + WS_G + (cg * S_ + T * 32) * 4, r * 4, __builtin_amdgcn_rcpf(1.0f + EXP2(-LOG2E * gacc)));
            }
        };
        if (ch) qkv(std::integral_constant<int, 2>{});
        else if (odd) qkv(std::integral_constant<int, 1>{});
        else qkv(std::integral_constant<int, 0>{});
    }
    const int head = xcd * 4 + (idx & 3), qb = idx >> 2;
    const int AT = qb * 8 + wid;
    const int krot = KROT * 8 * qb;
    const char* kf = ws + WS_K + (size_t)head * NTT * 1024;
    const char* vf = ws + WS_V + (size_t)head * NTT * 1024;
    const bool isv = blk == 0;
    const unsigned voff = h * 256 + (lane & 15) * 16;
    const u32x4 ones = {0x3f803f80u, 0x3f803f80u, 0x3f803f80u, 0x3f803f80u};
    const int mb = xcd * 4 + (idx & 3), nb = idx >> 2;
    const int mh = wid & 1, kq = wid >> 1;
    bf16x8 qf, k0, kk, g0, g1;
    float agate, tgate;
    unsigned ldv = lds0 + wid * 1040, ldk = lds0 + wid * 1024;
    SPIN(ldv); SPIN(ldk);
    auto issue_kv = [&](int c, int op) {
        if (op == 0) glds16_sc1(vf + ((8 * c + 1 + wid + krot) & 63) * 1024, loff, ldv, c * CHK);
        else glds16_sc1(kf + ((8 * c + 2 + wid + krot) & 63) * 1024, loff, ldk, c * CHK + KOFF);
    };
    f32x16 o, acc[4];
    bf16x8 ha[3], hb[3];
    {
        f32x16 fa[3] = {};
        u64 pollv = 0;
        const char* fq = ws + WS_FQ + xcd * 256;
        STAMP(12);
#pragma unroll
        for (int g = 0; g < 32; ++g) {
            const int s = 8 + ((g + 2) >> 2), op = (g + 2) & 3;
            if (op == 0 && s <= 14) {
                if (s == 9) wait_vm<12>();
                else if (s == 10) wait_vm<4>();
                else if (s == 12) wait_vm<9>();
                else if (s == 13) wait_vm<6>();
                else if (s == 14) {
                    asm volatile("s_waitcnt vmcnt(0)" : "+v"(pollv) :: "memory");
                    if (wid == 0 && !__all(pollv == tag)) spin_flags(fq + r * 8, true, tag);
                }
                bar();
                STAMP(4 + s);
                if (s == 10) flag_store(ws + WS_FQ + hg * 256 + (odd * 16 + rb) * 8, 0, tag, lead);
            }
            if (g < 30) frd_(g + 2);
            SBAR();
#pragma unroll
            for (int u = 0; u < 3; ++u) fa[u] = MFMA(fw[g % 3][u], xr[g], fa[u]);
            PIN(fa[0]); PIN(fa[1]); PIN(fa[2]);
            SBAR();
            if (s <= 11 && !NODMA) issue_f(s + 4, op);
            if (g == POLLG) asm volatile("global_load_dwordx2 %0, %1, %2 sc1" : "+v"(pollv) : "v"(r * 8), "s"(fq) : "memory");
            if (g == 22) { gld16_sc1(qf, ws + WS_Q + ((size_t)head * NTT + AT) * 1024, loff); gld16_sc1(k0, kf + krot * 1024, loff); }
            if (g == 23) { gld16_sc1(g0, vf + krot * 1024, voff); gld16_sc1(g1, vf + krot * 1024 + 512, voff); }
            if (g == 24) { gld16_sc1(kk, kf + (krot + 1) * 1024, loff); issue_kv(0, 0); }
            if (g == 25) { issue_kv(0, 1); gld4_sc1(agate, ws + WS_G + (2 * xcd * S_ + AT * 32) * 4, r * 4); }
            if (g >= 26) issue_kv(1 + (g - 26) / 2, g & 1);
            if (g == 26) gld4_sc1(tgate, ws + WS_G + (2 * xcd * S_ + (2 * mb + mh) * 32) * 4, r * 4);
            if (g == 22) *(LDSP u32x4*)((ldsptr)smem + min(wid, 5) * CHK + (lane & 7) * 1040 + 1024) = ones;
            if (g >= 24 && g < 28) { acc[g - 24] = f32x16{}; PIN(acc[g - 24]); }
            if (g == 28) { o = f32x16{}; PIN(o); }
            if (g >= 29) { ha[g - 29] = bf16x8{}; hb[g - 29] = bf16x8{}; PIN(ha[g - 29]); PIN(hb[g - 29]); }
            SBAR();
        }
        STAMP(20);
        char* hp = ws + WS_H + ((size_t)T * NKF + u0 + ch * 3) * 1024;
#pragma unroll
        for (int u = 0; u < 3; ++u) {
            f32x16 t;
#pragma unroll
            for (int j = 0; j < 8; ++j) {
                const float g = fa[u][j];
                t[j] = g * fa[u][8 + j] * __builtin_amdgcn_rcpf(1.0f + EXP2(-LOG2E * g));
            }
            const bool own = cg < 14 || (cg == 14 && ch * 3 + u >= 4);
            gst16_sc1(own ? hp + u * 1024 : ws + WS_DUMP + (blockIdx.x * 8 + wid) * 1024, loff, pack8(t, 0));
        }
    }
    const int fk0 = wid * 11 - (wid > 6 ? 1 : 0) - (wid < 6 ? 1 : 2);
    unsigned doff;
    {
        const int row = lane >> 3;
        doff = (unsigned)(row * 1024 + nb * 128 + (((lane & 7) ^ (((row >> 1) & 1) << 2)) * 16));
    }
    const int row0 = 4 * h + q, sw = (row0 >> 1) & 1;
    const int wfo0 = row0 * 128 + sw * 64 + (p4 & 1) * 32 + blk * 16 + (p4 >> 1) * 8, wfo1 = wfo0 ^ 64;
    const char* hfa = ws + WS_H + ((ptrdiff_t)(2 * mb + mh) * NKF + fk0) * 1024;
    const char* hfb = ws + WS_H + ((ptrdiff_t)(2 * mb + 1 - mh) * NKF + fk0) * 1024;
    const char* wdp = (const char*)wd + (ptrdiff_t)fk0 * 16384;
    {
        asm volatile("s_waitcnt vmcnt(11)" : "+v"(qf), "+v"(k0), "+v"(kk), "+v"(g0), "+v"(g1) :: "memory");
        STAMP(21);
        f32x16 z = {};
        f32x16 s = MFMA(k0, qf, z);
        bar();
        float m = fmaxf(s[0], s[1]);
#pragma unroll
        for (int i = 2; i < 16; i += 2) m = fmaxf(fmaxf(m, s[i]), s[i + 1]);
        m = fmaxf(m, __shfl_xor(m, 32));
        f32x16 negm;
#pragma unroll
        for (int i = 0; i < 16; ++i) negm[i] = -m;
        s = MFMA(k0, qf, negm);
        bf16x8 v0 = isv ? g0 : __builtin_bit_cast(bf16x8, ones), v1 = isv ? g1 : __builtin_bit_cast(bf16x8, ones);
        u32x4 p1 = {0, 0, 0, 0};
        bf16x8 v1p = v1;
        ldsptr krd = (ldsptr)smem + KOFF + lane * 16;
        ldsptr vrd0 = (ldsptr)smem + (isv ? voff : 1024);
        ldsptr vrd1 = (ldsptr)smem + (isv ? 512 + voff : 1024);
        ldsptr wr0 = (ldsptr)smem + wid * RING + wfo0, wr1 = (ldsptr)smem + wid * RING + wfo1;
        unsigned ringb = lds0 + wid * RING;
        SPIN(ringb);
        auto refill = [&](int e, const char* wsrc, int n, u64 mask) {
            glds16(wsrc, doff, ringb, e * 2048);
            glds16(wsrc + 8192, doff, ringb, e * 2048 + 1024);
            asm volatile("s_mov_b64 exec, %5\n\tglobal_load_dwordx4 %0, %2, %3 sc1\n\tglobal_load_dwordx4 %1, %2, %4 sc1\n\ts_mov_b64 exec, -1"
                         : "+v"(ha[e]), "+v"(hb[e]) : "v"(loff), "s"(hfa + n * 1024), "s"(hfb + n * 1024), "s"(mask) : "memory");
        };
        auto chunk = [&](auto fused_, const char* ws0, int wst0, int wst, int n0, u64 m0_, u64 m1_, u64 m2_) {
            constexpr bool FU = decltype(fused_)::value;
            bf16x8 wf;
#pragma unroll
            for (int i = 0; i < 8; ++i) {
                const int e = i / 3, ph = i % 3;
                SBAR();
                f32x16 sn = MFMA(kk, qf, negm);
                SBAR();
                kk = lds_b128(krd + i * 1024);
                bf16x8 nv0 = lds_b128(vrd0 + i * 1040);
                if (FU && ph == 0) wait_vm2<8>(ha[e], hb[e]);
                if (FU && ph < 2) wf = tr_frag((ph ? wr1 : wr0) + e * 2048, (ph ? wr1 : wr0) + e * 2048 + 1024);
                SBAR();
                u32x4 p0;
                { float a = EXP2(s[0]), b2 = EXP2(s[1]), c2 = EXP2(s[2]), d = EXP2(s[3]); p0[0] = cvtpk(a, b2); p0[1] = cvtpk(c2, d); }
                SBAR();
                o = MFMA(v1p, __builtin_bit_cast(bf16x8, p1), o);
                SBAR();
                v1p = v1;
                v1 = lds_b128(vrd1 + i * 1040);
                SBAR();
                { float a = EXP2(s[4]), b2 = EXP2(s[5]), c2 = EXP2(s[6]), d = EXP2(s[7]); p0[2] = cvtpk(a, b2); p0[3] = cvtpk(c2, d); }
                SBAR();
                float e8 = EXP2(s[8]), e9 = EXP2(s[9]);
                SBAR();
                o = MFMA(v0, __builtin_bit_cast(bf16x8, p0), o);
                SBAR();
                v0 = nv0;
                { float c2 = EXP2(s[10]), d = EXP2(s[11]); p1[0] = cvtpk(e8, e9); p1[1] = cvtpk(c2, d); }
                SBAR();
                if (FU && ph < 2) acc[ph] = MFMA(wf, ha[e], acc[ph]);
                SBAR();
                float e12 = EXP2(s[12]), e13 = EXP2(s[13]), e14 = EXP2(s[14]), e15 = EXP2(s[15]);
                SBAR();
                if (FU && ph < 2) acc[2 + ph] = MFMA(wf, hb[e], acc[2 + ph]);
                SBAR();
                p1[2] = cvtpk(e12, e13); p1[3] = cvtpk(e14, e15);
                SBAR();
                if (FU && (ph == 2 || i == 7)) refill(e, ws0 + (e == 0 ? wst0 : e == 1 ? 16384 : wst), n0 + e, e == 0 ? m0_ : e == 1 ? m1_ : m2_);
                SBAR();
                s = sn;
            }
            krd += CHK; vrd0 += CHK; vrd1 += CHK;
        };
        STAMP(22);
#pragma unroll 1
        for (int c = 0; c < 3; ++c) {
            if (c == 0) {
                issue_kv(4, 0); issue_kv(4, 1); issue_kv(5, 0); issue_kv(5, 1);
                if (wid == 7 && lane >= 48) *(LDSP u32x4*)((ldsptr)smem + (lane >> 3) * CHK + (lane & 7) * 1040 + 1024) = ones;
            } else if (c == 1) {
                wait_vm<4>();
                bar();
                flag_store(ws + WS_FH + rb * 128 + cg * 8, 0, tag, lead);
                issue_kv(6, 0); issue_kv(6, 1);
                if (wid < 7) issue_kv(7, 0);
                if (wid < 6) issue_kv(7, 1);
            } else {
#pragma unroll
                for (int e = 0; e < 4; ++e) glds16((const char*)wo + (wid * 4 + e) * 8192, doff, ldk * 4, WO_LO + e * 1024);
                if (wid == 0) glds4_sc1(ws + WS_FH + (mb >> 1) * 128, r * 4, lds0, POLL);
            }
            STAMP(30 + c);
            chunk(std::false_type{}, nullptr, 0, 0, 0, 0, 0, 0);
        }
        wait_vm<0>();
        if (wid == 0) {
            const u64 v = *(LDSP u64*)((ldsptr)smem + POLL + (lane & 15) * 8);
            if (!__all((lane & 15) == 15 || v == tag)) spin_flags(ws + WS_FH + (mb >> 1) * 128 + (lane & 15) * 8, (lane & 15) != 15, tag);
        }
        bar();
#pragma unroll 1
        for (int c = 3; c < 8; ++c) {
            STAMP(30 + c);
            const int n0 = c < 7 ? 3 * c - 9 : 2;
            const char* w0 = c < 7 ? wdp + n0 * 16384 : (const char*)wo + (16 + 2 * wid) * 16384;
            const u64 ml = c < 7 ? ~0ull : 1ull;
            chunk(std::true_type{}, w0, c == 3 ? 16384 : 0, c < 7 ? 32768 : 0, n0, c == 3 ? 0ull : ml, c == 3 && wid >= 6 ? 0ull : ml, ml);
        }
        o = MFMA(v1p, __builtin_bit_cast(bf16x8, p1), o);
        asm volatile("s_waitcnt vmcnt(0)" : "+v"(ha[0]), "+v"(hb[0]), "+v"(ha[1]), "+v"(hb[1]), "+v"(ha[2]), "+v"(hb[2]), "+v"(agate), "+v"(tgate) :: "memory");
    }
    STAMP(40);
    {
        float chk = 0.f;
#pragma unroll
        for (int i = 0; i < 8; ++i) chk += fabsf(o[i]) * 0.125f;
        const bool bad = !(o[8] <= 1.8446744e19f) || !(chk <= 3.0e38f);
        if (__builtin_expect(__any(bad), 0)) o = attn_fallback(kf, vf, qf, lane);
        const float rl = agate * __builtin_amdgcn_rcpf(o[8]);
        gst16_sc1(ws + WS_O + ((size_t)AT * NH + head) * 1024, loff, pack8(o, 0, rl));
        wait_vm<0>();
        bar();
        if (tid < 8) __hip_atomic_store((gu64*)(ws + WS_FO + tid * 2048 + qb * 256) + head, tag, RLX_AGENT);
    }
    STAMP(41);
    LDSP f32x4* xch = (LDSP f32x4*)((ldsptr)smem + XCH);
#pragma unroll
    for (int t = 0; t < 2; ++t)
#pragma unroll
        for (int i = 0; i < 4; ++i) {
            f32x4 v = {acc[2 + t][4 * i], acc[2 + t][4 * i + 1], acc[2 + t][4 * i + 2], acc[2 + t][4 * i + 3]};
            xch[wid * 512 + (t * 4 + i) * 64 + lane] = v;
        }
    bf16x8 wa[8][2];
    {
        const int base = kq < 2 ? WO_LO + kq * 16384 : (kq - 2) * 4 * RING, st2 = kq < 2 ? 4096 : RING;
#pragma unroll
        for (int j = 0; j < 8; ++j) {
            ldsptr p = (ldsptr)smem + base + (j >> 1) * st2 + (j & 1) * 2048;
            wa[j][0] = tr_frag(p + wfo0, p + wfo0 + 1024);
            wa[j][1] = tr_frag(p + wfo1, p + wfo1 + 1024);
        }
    }
    if (wid == 0) spin_flags(ws + WS_FO + nb * 2048 + (mb >> 2) * 256 + r * 8, true, tag);
    if (wid == 1 && blockIdx.x == 0) {
#pragma unroll
        for (int i = 0; i < 4; ++i) spin_flags(ws + WS_FQ + i * 512 + lane * 8, true, tag);
        if (lane == 0) __hip_atomic_store((gu64*)(ws + WS_EP), tag, RLX_AGENT);
    }
    bar();
    STAMP(42);
    const char* of = ws + WS_O + ((size_t)(2 * mb + mh) * NH + kq * 8) * 1024;
    bf16x8 oa[8];
    const float og = 1.0f - tgate;
#pragma unroll
    for (int j = 0; j < 8; ++j) gld16_sc1(oa[j], of + j * 1024, loff);
#pragma unroll
    for (int t = 0; t < 2; ++t)
#pragma unroll
        for (int i = 0; i < 4; ++i) {
            const f32x4 v = xch[(wid ^ 1) * 512 + (t * 4 + i) * 64 + lane];
#pragma unroll
            for (int j = 0; j < 4; ++j) acc[t][4 * i + j] = (acc[t][4 * i + j] + v[j]) * og;
        }
#pragma unroll
    for (int j = 0; j < 8; ++j) {
        if (j == 0) wait_vm1<7>(oa[j]); else if (j == 1) wait_vm1<6>(oa[j]); else if (j == 2) wait_vm1<5>(oa[j]);
        else if (j == 3) wait_vm1<4>(oa[j]); else if (j == 4) wait_vm1<3>(oa[j]); else if (j == 5) wait_vm1<2>(oa[j]);
        else if (j == 6) wait_vm1<1>(oa[j]); else wait_vm1<0>(oa[j]);
        acc[0] = MFMA(wa[j][0], oa[j], acc[0]);
        acc[1] = MFMA(wa[j][1], oa[j], acc[1]);
        SBAR();
    }
    STAMP(43);
#pragma unroll
    for (int t = 0; t < 2; ++t)
#pragma unroll
        for (int i = 0; i < 4; ++i) {
            f32x4 v = {acc[t][4 * i], acc[t][4 * i + 1], acc[t][4 * i + 2], acc[t][4 * i + 3]};
            xch[(wid ^ 1) * 512 + (t * 4 + i) * 64 + lane] = v;
        }
    bar();
    f32x4 ra = {}, rb4 = {};
#pragma unroll
    for (int k = 0; k < 4; ++k) {
        ra += xch[((2 * k + mh) ^ 1) * 512 + (kq * 2) * 64 + lane];
        rb4 += xch[((2 * k + mh) ^ 1) * 512 + (kq * 2 + 1) * 64 + lane];
    }
    const u32x4 ov = {cvtpk(ra[0], ra[1]), cvtpk(ra[2], ra[3]), cvtpk(rb4[0], rb4[1]), cvtpk(rb4[2], rb4[3])};
    {
        const unsigned oo = (mb * 64 + mh * 32 + r) * 1024 + (nb * 64 + (kq >> 1) * 32 + h * 16 + (kq & 1) * 8) * 2;
        asm volatile("global_store_dwordx4 %0, %1, %2 nt\n\ts_nop 1" :: "v"(oo), "v"(ov), "s"(out) : "memory");
    }
}

extern "C" void kernel_launch(void* const* d_in, const int* in_sizes, int n_in, void* d_out, int out_size, void* d_ws,
                              size_t ws_size, hipStream_t stream) {
    (void)hipFuncSetAttribute((const void*)fused, hipFuncAttributeMaxDynamicSharedMemorySize, LDS_BYTES);
    fused<<<256, 512, LDS_BYTES, stream>>>((const u16*)d_in[0], (const u16*)d_in[1], (const u16*)d_in[2], (const u16*)d_in[4],
                                           (const u16*)d_in[5], (const u16*)d_in[6], (const u16*)d_in[7], (char*)d_ws, (u16*)d_out);
}
```

```cpp
#define WSOFF ((ws_size - (16u << 20)) & ~(size_t)4095)
#include <hip/hip_runtime.h>
#include <hip/hip_bf16.h>
#include <stdint.h>
#include <type_traits>

typedef unsigned short u16;
typedef unsigned long long u64;
typedef __attribute__((ext_vector_type(8))) __bf16 bf16x8;
typedef __attribute__((ext_vector_type(2))) __bf16 bf16x2;
typedef __attribute__((ext_vector_type(4))) short s16x4;
typedef __attribute__((ext_vector_type(16))) float f32x16;
typedef __attribute__((ext_vector_type(4))) float f32x4;
typedef __attribute__((ext_vector_type(2))) float f32x2;
typedef __attribute__((ext_vector_type(4))) unsigned u32x4;
#define LDSP __attribute__((address_space(3)))
typedef LDSP char* ldsptr;
typedef __attribute__((address_space(1))) unsigned gu32;
typedef __attribute__((address_space(1))) u64 gu64;

#define FPOS 8
#define G9 26
constexpr int S_ = 2048, F_ = 1376, NH = 32;
constexpr int NTT = S_ / 32;
constexpr int NKF = F_ / 16;
constexpr float LOG2E = 1.4426950408889634f;
constexpr float QSCALE = 0.25f * LOG2E;

constexpr size_t WS_EP = 0;
constexpr size_t WS_FQ = 1024;
constexpr size_t WS_FH = WS_FQ + 2048;
constexpr size_t WS_FO = WS_FH + 2048;
constexpr size_t WS_G = WS_FO + 16384;
constexpr size_t WS_Q = 262144;
constexpr size_t WS_K = WS_Q + (size_t)NH * NTT * 1024;
constexpr size_t WS_V = WS_K + (size_t)NH * NTT * 1024;
constexpr size_t WS_O = WS_V + (size_t)NH * NTT * 1024;
constexpr size_t WS_H = WS_O + (size_t)NTT * 32 * 1024;

constexpr size_t WS_DUMP = WS_H + (size_t)NTT * NKF * 1024;

constexpr int LDS_BYTES = 163840;
constexpr int SLOT = 28672;
__host__ __device__ constexpr int phys(int st) { return 20480 + (st < 8 && FPOS <= 4 ? (3 + (st & 3)) % 5 : (st + 4) % 5) * SLOT; }
constexpr int F1OFF = 4064, F1UP = 12320;
constexpr int CHK = 16512, KOFF = 8320;
constexpr int WO_LO = 130048, POLL = 162816, XCH = 49536, RING = 6144;

#define MFMA(a, b, c) __builtin_amdgcn_mfma_f32_32x32x16_bf16((a), (b), (c), 0, 0, 0)
#define ROT 1
#define KROT 1
#define IBAR 4
#define NI 2
#define RATE 2
#define TPF 0
#define TPW 0
#define FLS 11
#define PB 22
#define POLLG (PB - 4)
#define SPIN(x) asm volatile("" : "+s"(x))
#define SBAR() __builtin_amdgcn_sched_barrier(0)
#define EXP2(x) __builtin_amdgcn_exp2f(x)
#define RLX_AGENT __ATOMIC_RELAXED, __HIP_MEMORY_SCOPE_AGENT
#define STAMP(i)
#define STAMPC(i)

__device__ __forceinline__ unsigned cvtpk(float lo, float hi) {
    f32x2 v = {lo, hi};
    return __builtin_bit_cast(unsigned, __builtin_convertvector(v, bf16x2));
}
__device__ __forceinline__ void glds16(const void* sbase, unsigned voff, unsigned lds_s, int lds_i) {
    asm volatile("s_add_u32 m0, %2, %3\n\ts_nop 0\n\tglobal_load_lds_dwordx4 %0, %1" :: "v"(voff), "s"(sbase), "s"(lds_s), "i"(lds_i) : "memory", "scc");
}
__device__ __forceinline__ void glds16k(unsigned blo, unsigned bhi, int k, int stride, unsigned voff, unsigned lds_s, int lds_i) {
    asm volatile("s_add_u32 m0, %1, %2\n\ts_mul_i32 vcc_lo, %3, %4\n\ts_add_u32 vcc_lo, vcc_lo, %5\n\ts_addc_u32 vcc_hi, %6, 0\n\tglobal_load_lds_dwordx4 %0, vcc"
                 :: "v"(voff), "s"(lds_s), "i"(lds_i), "s"(k), "i"(stride), "s"(blo), "s"(bhi) : "memory", "scc", "vcc");
}
__device__ __forceinline__ void glds16_sc1(const void* sbase, unsigned voff, unsigned lds_s, int lds_i) {
    asm volatile("s_add_u32 m0, %2, %3\n\ts_nop 0\n\tglobal_load_lds_dwordx4 %0, %1 sc1" :: "v"(voff), "s"(sbase), "s"(lds_s), "i"(lds_i) : "memory", "scc");
}
__device__ __forceinline__ void glds4_sc1(const void* sbase, unsigned voff, unsigned lds_s, int lds_i) {
    asm volatile("s_add_u32 m0, %2, %3\n\ts_nop 0\n\tglobal_load_lds_dword %0, %1 sc1" :: "v"(voff), "s"(sbase), "s"(lds_s), "i"(lds_i) : "memory", "scc");
}
__device__ __forceinline__ void gld16_sc1(bf16x8& r, const void* sbase, unsigned voff) {
    asm volatile("global_load_dwordx4 %0, %1, %2 sc1" : "=v"(r) : "v"(voff), "s"(sbase) : "memory");
}
__device__ __forceinline__ void gld16_sc1_tied(bf16x8& r, const void* sbase, unsigned voff) {
    asm volatile("global_load_dwordx4 %0, %1, %2 sc1" : "+v"(r) : "v"(voff), "s"(sbase) : "memory");
}
__device__ __forceinline__ void gld8_sc1(f32x2& r, const void* sbase, unsigned voff) {
    asm volatile("global_load_dwordx2 %0, %1, %2 sc1" : "=v"(r) : "v"(voff), "s"(sbase) : "memory");
}
__device__ __forceinline__ void gst16_sc1(void* sbase, unsigned voff, bf16x8 v) {
    asm volatile("global_store_dwordx4 %0, %1, %2 sc1\n\ts_nop 1" :: "v"(voff), "v"(v), "s"(sbase) : "memory");
}
__device__ __forceinline__ void gst4_sc1(void* sbase, unsigned voff, float v) {
    asm volatile("global_store_dword %0, %1, %2 sc1" :: "v"(voff), "v"(v), "s"(sbase) : "memory");
}
template <int N> __device__ __forceinline__ void wait_vm() { asm volatile("s_waitcnt vmcnt(%0)" ::"i"(N) : "memory"); }
template <int N> __device__ __forceinline__ void wait_vm1(bf16x8& a) { asm volatile("s_waitcnt vmcnt(%1)" : "+v"(a) : "i"(N) : "memory"); }
template <int N> __device__ __forceinline__ void wait_vm2(bf16x8& a, bf16x8& b) {
    asm volatile("s_waitcnt vmcnt(%2)" : "+v"(a), "+v"(b) : "i"(N) : "memory");
}
__device__ __forceinline__ void bar() { asm volatile("s_waitcnt lgkmcnt(0)\n\ts_barrier" ::: "memory"); }
__device__ __forceinline__ ldsptr ladd(ldsptr p, int off) {
    unsigned r;
    asm volatile("v_add_u32 %0, %1, %2" : "=v"(r) : "i"(off), "v"((unsigned)(uintptr_t)p));
    return (ldsptr)(uintptr_t)r;
}
__device__ __forceinline__ bf16x8 lds_b128(ldsptr p) { return *(LDSP bf16x8*)p; }
__device__ __forceinline__ bf16x8 tr_frag(ldsptr p0, ldsptr p1) {
    s16x4 a = __builtin_amdgcn_ds_read_tr16_b64_v4i16((LDSP s16x4*)p0), b = __builtin_amdgcn_ds_read_tr16_b64_v4i16((LDSP s16x4*)p1);
    typedef __attribute__((ext_vector_type(8))) short s16x8;
    s16x8 v = {a[0], a[1], a[2], a[3], b[0], b[1], b[2], b[3]};
    return __builtin_bit_cast(bf16x8, v);
}
__device__ __forceinline__ bf16x8 pack8(const f32x16& a, int o, float sc) {
    u32x4 v = {cvtpk(a[o] * sc, a[o + 1] * sc), cvtpk(a[o + 2] * sc, a[o + 3] * sc),
               cvtpk(a[o + 4] * sc, a[o + 5] * sc), cvtpk(a[o + 6] * sc, a[o + 7] * sc)};
    return __builtin_bit_cast(bf16x8, v);
}
__device__ __forceinline__ bf16x8 pack8(const f32x16& a, int o) {
    u32x4 v = {cvtpk(a[o], a[o + 1]), cvtpk(a[o + 2], a[o + 3]), cvtpk(a[o + 4], a[o + 5]), cvtpk(a[o + 6], a[o + 7])};
    return __builtin_bit_cast(bf16x8, v);
}
__device__ __forceinline__ void spin_flags(const char* p, bool need, u64 tag) {
#pragma unroll 1
    for (int spin = 0; spin < (1 << 20); ++spin) {
        const u64 v = __hip_atomic_load((gu64*)p, RLX_AGENT);
        if (__all(!need || v == tag)) break;
        __builtin_amdgcn_s_sleep(2);
    }
}

__host__ __device__ constexpr int item(int p) { return p < FPOS ? p : p == FPOS ? 8 : p <= 8 ? p - 1 : p; }
__host__ __device__ constexpr int pos(int s) { return s < FPOS ? s : s == 8 ? FPOS : s < 8 ? s + 1 : s; }
__host__ __device__ constexpr int endp(int p, int nq) {
    int n = 0;
    for (int i = 0; i <= p; ++i) n += item(i) < 8 ? nq : 3;
    return n;
}
__host__ __device__ constexpr int issued(int g, int nq) {
    int n = endp(NI - 1, nq);
    for (int i = 0; i < g; ++i) {
        const int b = (i + 2) >> 2, per = item(b + 4) < 8 ? nq : 3, op = ((i + 2) & 3) + 1;
        int t = b == 0 ? endp(4, nq) : endp(b + 3, nq) + (op < per ? op : per);
        const int cap = i < G9 ? endp(8, nq) : endp(15, nq);
        if (t > cap) t = cap;
        n = n + RATE < t ? n + RATE : t;
    }
    return n;
}
__host__ __device__ constexpr int posof(int n, int nq) {
    int p = 0;
    while (endp(p, nq) <= n) ++p;
    return p;
}
template <int NQ, int S, int G = (S == 0 ? 0 : 4 * S - 2)> __device__ __forceinline__ void wait_st() {
    constexpr int n = issued(G, NQ) - endp(pos(S), NQ) + (G >= 32 && endp(pos(S), NQ) <= issued(32, NQ) ? 3 : 0);
    static_assert(issued(G, NQ) >= endp(pos(S), NQ) && n < 64, "stage not issued");
    wait_vm<n>();
}
template <int I, int N, class F> __device__ __forceinline__ void sfor(F&& f) {
    if constexpr (I < N) { f(std::integral_constant<int, I>{}); sfor<I + 1, N>(f); }
}
static_assert(issued(32, 3) - endp(8, 3) == issued(32, 4) - endp(8, 4) && issued(32, 3) >= endp(8, 3), "common schedule from k-step 32 on");
__device__ __forceinline__ void flag_store(void* sbase, unsigned voff, u64 v, unsigned mask) {
    asm volatile("s_mov_b32 exec_lo, %3\n\ts_mov_b32 exec_hi, 0\n\tglobal_store_dwordx2 %0, %1, %2 sc1\n\ts_mov_b64 exec, -1" :: "v"(voff), "v"(v), "s"(sbase), "s"(mask) : "memory");
}
#define PIN(x) asm volatile("" : "+v"(x))
#define LAD 1
#define CPF 1
#define FDO 0
#define FCO 5
#define FDY 3
#define FCY 7
template <int MASK> __device__ __forceinline__ void drop_if(int key) {
    if (LAD) asm volatile("s_bitcmp0_b32 %1, %0\n\ts_cbranch_scc1 1f\n\ts_setprio 0\n1:" :: "s"(key), "i"(MASK) : "scc");
}

__device__ __forceinline__ f32x16 attn_fallback(const char* kf, const char* vf, bf16x8 qf, int lane) {
    f32x16 o = {};
    float m = -INFINITY;
    const bool isv = ((lane >> 4) & 1) == 0;
    const u32x4 ones = {0x3f803f80u, 0x3f803f80u, 0x3f803f80u, 0x3f803f80u};
    const unsigned voff = (lane >> 5) * 256 + (lane & 15) * 16;
#pragma unroll 1
    for (int t = 0; t < NTT; ++t) {
        bf16x8 kk, a0, a1;
        gld16_sc1(kk, kf + t * 1024, lane * 16);
        gld16_sc1(a0, vf + t * 1024, voff);
        gld16_sc1(a1, vf + t * 1024 + 512, voff);
        wait_vm1<0>(kk);
        wait_vm2<0>(a0, a1);
        u32x4 v0 = __builtin_bit_cast(u32x4, a0), v1 = __builtin_bit_cast(u32x4, a1);
        if (!isv) { v0 = ones; v1 = ones; }
        f32x16 z = {};
        f32x16 s = MFMA(kk, qf, z);
        float tm = s[0];
#pragma unroll
        for (int i = 1; i < 16; ++i) tm = fmaxf(tm, s[i]);
        tm = fmaxf(tm, __shfl_xor(tm, 32));
        const float mn = fmaxf(m, tm);
        const float al = EXP2(m - mn);
        m = mn;
#pragma unroll
        for (int i = 0; i < 16; ++i) { o[i] *= al; s[i] = EXP2(s[i] - mn); }
        o = MFMA(__builtin_bit_cast(bf16x8, v0), pack8(s, 0), o);
        o = MFMA(__builtin_bit_cast(bf16x8, v1), pack8(s, 8), o);
    }
    return o;
}

__global__ __launch_bounds__(512, 2) void fused(const u16* __restrict__ x_, const u16* __restrict__ wqkv_, const u16* __restrict__ wo_,
                                                const u16* __restrict__ wg_, const u16* __restrict__ wu_, const u16* __restrict__ wd_,
                                                const u16* __restrict__ gatew_, char* __restrict__ ws_a, u16* __restrict__ out_) {
    extern __shared__ __attribute__((aligned(16))) char smem[];
    STAMP(0); STAMPC(61);
    typedef __attribute__((ext_vector_type(8))) u64 u64x8;
    u64x8 ka;
    u64 kb;
    asm volatile("s_load_dwordx16 %0, %2, 0\n\ts_load_dwordx2 %1, %2, 0x40" : "=&s"(ka), "=&s"(kb) : "s"(__builtin_amdgcn_kernarg_segment_ptr()));
    const int tid = threadIdx.x, lane = tid & 63;
    const int wid = __builtin_amdgcn_readfirstlane(tid >> 6);
    const int xcd = blockIdx.x & 7, idx = blockIdx.x >> 3;
    const unsigned lds0 = 0;
    const unsigned loff = lane * 16;
    if (CPF) {
        u64 pc;
        unsigned len;
        asm volatile("s_getpc_b64 %0\n.Lcode_begin:\n\ts_mov_b32 %1, .Lcode_end-.Lcode_begin-4" : "=s"(pc), "=s"(len));
        const unsigned line = min((unsigned)(((idx % 3) * 128 + wid * 16 + (lane & 15)) * 128), len);
        asm volatile("s_mov_b32 m0, %2\n\ts_nop 0\n\tglobal_load_lds_dword %0, %1" :: "v"(line), "s"(pc), "i"(4096) : "memory");
    }
    const int r = lane & 31, h = lane >> 5, q = (lane & 15) >> 2, p4 = lane & 3, blk = (lane >> 4) & 1;
    const int rb = (xcd & 1) * 8 + (idx & 7), cg = (xcd >> 1) * 4 + (idx >> 3);
    const int tt = wid & 3, ch = wid >> 2, hg = cg >> 1, odd = cg & 1;
    const int rot = ROT * ((idx & 7) + 2 * (idx >> 3));
    unsigned xoff[2], woff[2], foff[3];
#pragma unroll
    for (int e = 0; e < 2; ++e) {
        const int row = wid * 16 + e * 8 + (lane >> 3);
        xoff[e] = (unsigned)((rb * 128 + row) * 1024 + (((lane & 7) ^ ((row >> 1) & 7) ^ (odd * 4)) * 16));
    }
    const int wp0 = ch ? 2 * wid - 4 : wid;
#pragma unroll
    for (int e = 0; e < 2; ++e) {
        const int n = (wp0 + e) * 64 + lane, kr = n / 12, c = n % 12;
        woff[e] = (unsigned)((kr ^ (odd * 32)) * 3072 + hg * 128 + (c < 8 ? odd * 2048 + c * 16 : 1024 - 128 + odd * 64 + c * 16));
    }
    unsigned ldx = lds0 + wid * 2048, ldw = lds0 + wp0 * 1024;
    unsigned fdst = lds0 + F1OFF + ch * F1UP + tt * 3072;
    SPIN(ldx); SPIN(ldw); SPIN(fdst);
    int kbs[8];
#pragma unroll
    for (int i = 0; i < 8; ++i) { kbs[i] = (i + rot) & 7; SPIN(kbs[i]); }
    const int u0 = cg < 14 ? cg * 6 : 80;
#pragma unroll
    for (int e = 0; e < 3; ++e) {
        const int n = (tt * 3 + e) * 64 + lane, kr = n / 12, c = n % 12;
        foff[e] = (unsigned)((kr ^ (odd * 32)) * (F_ * 2) + u0 * 32 + c * 16);
    }
    unsigned goff = ((loff ^ (odd * 64)) + rot * 128) & 1023;
    PIN(xoff[0]); PIN(xoff[1]); PIN(woff[0]); PIN(woff[1]); PIN(foff[0]); PIN(foff[1]); PIN(foff[2]); PIN(goff);
    asm volatile("s_waitcnt lgkmcnt(0)" : "+s"(ka), "+s"(kb));
    STAMP(59);
    const char* wo = (const char*)ka[2];
    const char* wd = (const char*)ka[5];
    char* ws = (char*)ka[7];
    char* out = (char*)kb;
    u64 ep;
    asm volatile("s_load_dwordx2 %0, %1, 0" : "=&s"(ep) : "s"(ws + WS_EP));
    if (TPF && wid == TPW) {
        const int a = idx & 7;
        const u64 base = a == 0 ? ka[0] : a == 1 ? ka[1] : a == 2 ? ka[2] : a == 3 ? ka[3] : a == 4 ? ka[4] : a == 5 ? ka[5] : a == 6 ? kb : ka[7];
        const unsigned step = a == 7 ? (1u << 20) : a == 2 ? (1u << 15) : a == 1 ? 98304u : (a == 0 || a == 6) ? (1u << 17) : 88064u;
        const unsigned off = ((idx >> 3) * 4 + (lane & (TPF - 1)) * (4 / TPF)) * step;
        asm volatile("s_mov_b32 m0, %2\n\ts_nop 0\n\tglobal_load_lds_dword %0, %1" :: "v"(off), "s"(base), "i"(4096) : "memory");
    }
    const u64 fsrc = ch ? ka[4] : ka[3];
    unsigned xlo = ka[0], xhi = ka[0] >> 32, wlo = ka[1], whi = ka[1] >> 32, flo = fsrc, fhi = fsrc >> 32;
    SPIN(xlo); SPIN(xhi); SPIN(wlo); SPIN(whi); SPIN(flo); SPIN(fhi);
    auto issue_q = [&](int st, int op) {
        if (op == 0) glds16k(xlo, xhi, kbs[st], 128, xoff[0], ldx, phys(st));
        if (op == 1) glds16k(wlo, whi, kbs[st], 64 * 3072, woff[0], ldw, phys(st) + 16384);
        if (op == 2) glds16k(xlo, xhi, kbs[st], 128, xoff[1], ldx, phys(st) + 1024);
        if (op == 3) glds16k(wlo, whi, kbs[st], 64 * 3072, woff[1], ldw, phys(st) + 16384 + 1024);
    };
    auto issue_f = [&](int st, int op) { glds16k(flo, fhi, kbs[st & 7], 64 * F_ * 2, foff[op], fdst, phys(st) + op * 1024); };
    if (wid == 3) glds16((const char*)ka[6], goff, lds0, 0);
#pragma unroll
    for (int st = 0; st < NI; ++st) {
        if (item(st) == 8) { issue_f(8, 0); issue_f(8, 1); issue_f(8, 2); }
        else {
            issue_q(item(st), 0); issue_q(item(st), 1); issue_q(item(st), 2);
            if (ch) issue_q(item(st), 3);
        }
        if (st < IBAR && st < NI - 1) asm volatile("s_barrier" ::: "memory");
    }
    STAMP(1);
    asm volatile("s_waitcnt lgkmcnt(0)" : "+s"(ep));
    const u64 tag = (ep ^ (ep >> 31)) * 0x9E3779B97F4A7C15ull + 0xD1B54A32D192ED03ull;
    unsigned lead = __builtin_amdgcn_readfirstlane(tid < 64 ? 1 : 0);
    SPIN(lead);
    bf16x8 xr[32];
    const int T = rb * 4 + tt;
    bf16x8 fw[3][3];
    ldsptr frd = (ldsptr)smem + F1OFF + (8 * h + q) * 192 + blk * F1UP + p4 * 8 + ch * 96, fs_;
    auto frd_ = [&](int g) {
        if ((g & 3) == 0) fs_ = ladd(frd, phys(8 + (g >> 2)));
        const int so = (g & 3) * 3072;
#pragma unroll
        for (int u = 0; u < 3; ++u) fw[g % 3][u] = tr_frag(fs_ + so + u * 32, fs_ + so + u * 32 + 768);
    };
    {
        ldsptr xrd[4];
#pragma unroll
        for (int ks = 0; ks < 4; ++ks) xrd[ks] = (ldsptr)smem + (tt * 32 + r) * 128 + (((2 * ks + h) ^ ((r >> 1) & 7)) * 16);
        ldsptr wrd = (ldsptr)smem + 16384 + (8 * h + q) * 192 + blk * 32 + p4 * 8 + ch * 128;
        ldsptr grd = (ldsptr)smem + h * 16;
        auto qkv = [&](auto var) {
            constexpr int VAR = decltype(var)::value;
            constexpr int NQ = VAR == 2 ? 4 : 3;
            f32x16 a0 = {}, a1 = {};
            bf16x8 w0[3], w1[3];
            ldsptr ws_;
            auto rd = [&](int g) {
                const int so = phys(g >> 2), ks = g & 3;
                if (ks == 0) ws_ = ladd(wrd, so);
                xr[g] = lds_b128(ladd(xrd[ks], so));
                w0[g % 3] = tr_frag(ws_ + ks * 3072, ws_ + ks * 3072 + 768);
                if (VAR != 2) w1[g % 3] = tr_frag(ws_ + ks * 3072 + 64, ws_ + ks * 3072 + 64 + 768);
                else if (ks < 2) w1[g % 3] = lds_b128(grd + g * 32);
            };
            wait_st<NQ, 0>();
            bar();
            STAMP(2);
            rd(0);
            rd(1);
            sfor<0, 32>([&](auto g_) {
                constexpr int g = decltype(g_)::value, s = (g + 2) >> 2, op = (g + 2) & 3;
                if constexpr (op == 0) {
                    wait_st<NQ, s>();
                    bar();
                    STAMP(2 + s);
                }
                if (g < 30) rd(g + 2); else frd_(g - 30);
                SBAR();
                if (VAR == 1) { a0 = MFMA(xr[g], w0[g % 3], a0); a1 = MFMA(xr[g], w1[g % 3], a1); }
                else { a0 = MFMA(w0[g % 3], xr[g], a0); if (VAR == 0 || (g & 3) < 2) a1 = MFMA(w1[g % 3], xr[g], a1); }
                PIN(a0); PIN(a1);
                SBAR();
                sfor<issued(g, NQ), issued(g + 1, NQ)>([&](auto n_) {
                    constexpr int n = decltype(n_)::value, ps = posof(n, NQ), st = item(ps), o_ = n - endp(ps - 1, NQ);
                    if (st < 8) issue_q(st, o_); else issue_f(st, o_);
                });
                SBAR();
            });
            STAMP(11);
            if (VAR == 0) {
                char* qp = ws + WS_Q + ((size_t)(4 * hg) * NTT + T) * 1024;
                gst16_sc1(qp, loff, pack8(a0, 0, QSCALE));
                gst16_sc1(qp + NTT * 1024, loff, pack8(a0, 8, QSCALE));
                gst16_sc1(qp + 2 * NTT * 1024, loff, pack8(a1, 0, QSCALE));
                gst16_sc1(qp + 3 * NTT * 1024, loff, pack8(a1, 8, QSCALE));
            } else if (VAR == 1) {
                char* vp = ws + WS_V + ((size_t)(4 * hg) * NTT + T) * 1024;
                const unsigned vo = blk * (NTT * 1024) + h * 256 + (lane & 15) * 16;
                gst16_sc1(vp, vo, pack8(a0, 0));
                gst16_sc1(vp + 512, vo, pack8(a0, 8));
                gst16_sc1(vp + 2 * NTT * 1024, vo, pack8(a1, 0));
                gst16_sc1(vp + 2 * NTT * 1024 + 512, vo, pack8(a1, 8));
            } else {
                char* kp = ws + WS_K + ((size_t)(4 * hg + 2 * odd) * NTT + T) * 1024;
                gst16_sc1(kp, loff, pack8(a0, 0));
                gst16_sc1(kp + NTT * 1024, loff, pack8(a0, 8));
                gst4_sc1(ws + WS_G + (hg * S_ + T * 32) * 8 + odd * 4, r * 8, a1[0]);
            }
        };
        if (ch) qkv(std::integral_constant<int, 2>{});
        else if (odd) qkv(std::integral_constant<int, 1>{});
        else qkv(std::integral_constant<int, 0>{});
    }
    const int head = xcd * 4 + (idx & 3), qb = idx >> 2;
    const int AT = qb * 8 + wid;
    const int krot = KROT * 8 * qb;
    const char* kf = ws + WS_K + (size_t)head * NTT * 1024;
    const char* vf = ws + WS_V + (size_t)head * NTT * 1024;
    const bool isv = blk == 0;
    const unsigned voff = h * 256 + (lane & 15) * 16;
    const u32x4 ones = {0x3f803f80u, 0x3f803f80u, 0x3f803f80u, 0x3f803f80u};
    const int mb = xcd * 4 + (idx & 3), nb = idx >> 2;
    const int mh = wid & 1, kq = wid >> 1;
    bf16x8 qf, k0, kk, g0, g1;
    f32x2 agate, tgate;
    unsigned ldv = lds0 + wid * 1040, ldk = lds0 + wid * 1024;
    SPIN(ldv); SPIN(ldk);
    auto issue_kv = [&](int c, int op) {
        if (op == 0) glds16_sc1(vf + ((8 * c + 1 + wid + krot) & 63) * 1024, loff, ldv, c * CHK);
        else glds16_sc1(kf + ((8 * c + 2 + wid + krot) & 63) * 1024, loff, ldk, c * CHK + KOFF);
    };
    f32x16 o, acc[4];
    bf16x8 ha[3], hb[3];
    {
        f32x16 fa[3] = {};
        u64 pollv = 0;
        const char* fq = ws + WS_FQ + xcd * 256;
        STAMP(12);
        sfor<0, 32>([&](auto g_) {
            constexpr int g = decltype(g_)::value, s = 8 + ((g + 2) >> 2), op = (g + 2) & 3;
            if constexpr (op == 0 && s <= 13) {
                if constexpr (s == FLS) wait_vm<issued(32 + g, 3) - issued(32, 3)>();
                if constexpr (s == 13) wait_st<3, 15, 32 + g>(); else wait_st<3, s>();
                bar();
                STAMP(4 + s);
                if (s == FLS) flag_store(ws + WS_FQ + hg * 256 + (odd * 16 + rb) * 8, 0, tag, lead);
            }
            if (g == PB) {
                asm volatile("s_waitcnt vmcnt(0)" : "+v"(pollv) :: "memory");
                if (__builtin_expect(wid == 0 && !__all(pollv == tag), 0)) spin_flags(fq + r * 8, true, tag);
                bar();
                STAMP(18);
            }
            if (g < 30) frd_(g + 2);
            SBAR();
#pragma unroll
            for (int u = 0; u < 3; ++u) fa[u] = MFMA(fw[g % 3][u], xr[g], fa[u]);
            PIN(fa[0]); PIN(fa[1]); PIN(fa[2]);
            SBAR();
            sfor<issued(32 + g, 3), issued(33 + g, 3)>([&](auto n_) { constexpr int n = decltype(n_)::value; issue_f(n / 3, n % 3); });
            if (g == POLLG) asm volatile("global_load_dwordx2 %0, %1, %2 sc1" : "+v"(pollv) : "v"(r * 8), "s"(fq) : "memory");
            if (g == PB) { gld16_sc1(qf, ws + WS_Q + ((size_t)head * NTT + AT) * 1024, loff); gld16_sc1(k0, kf + krot * 1024, loff); }
            if (g == PB + 1) { gld16_sc1(g0, vf + krot * 1024, voff); gld16_sc1(g1, vf + krot * 1024 + 512, voff); }
            if (g == PB + 2) { gld16_sc1(kk, kf + (krot + 1) * 1024, loff); issue_kv(0, 0); }
            if (g == PB + 3) { issue_kv(0, 1); gld8_sc1(agate, ws + WS_G + (xcd * S_ + AT * 32) * 8, r * 8); }
            if (g == PB + 4) gld8_sc1(tgate, ws + WS_G + (xcd * S_ + (2 * mb + mh) * 32) * 8, r * 8);
#pragma unroll
            for (int n = 0; n < 6; ++n)
                if (g == PB + 4 + n * (28 - PB) / 6) issue_kv(1 + n / 2, n & 1);
            if (g == PB) *(LDSP u32x4*)((ldsptr)smem + min(wid, 5) * CHK + (lane & 7) * 1040 + 1024) = ones;
            if (g >= 24 && g < 28) { acc[g - 24] = f32x16{}; PIN(acc[g - 24]); }
            if (g == 28) { o = f32x16{}; PIN(o); }
            if (g >= 29) { ha[g - 29] = bf16x8{}; hb[g - 29] = bf16x8{}; PIN(ha[g - 29]); PIN(hb[g - 29]); }
            SBAR();
        });
        STAMP(20);
        char* hp = ws + WS_H + ((size_t)T * NKF + u0 + ch * 3) * 1024;
#pragma unroll
        for (int u = 0; u < 3; ++u) {
            f32x16 t;
#pragma unroll
            for (int j = 0; j < 8; ++j) {
                const float g = fa[u][j];
                t[j] = g * fa[u][8 + j] * __builtin_amdgcn_rcpf(1.0f + EXP2(-LOG2E * g));
            }
            const bool own = cg < 14 || (cg == 14 && ch * 3 + u >= 4);
            gst16_sc1(own ? hp + u * 1024 : ws + WS_DUMP + (blockIdx.x * 8 + wid) * 1024, loff, pack8(t, 0));
        }
    }
    const int fk0 = wid * 11 - (wid > 6 ? 1 : 0) - (wid < 6 ? 1 : 2);
    unsigned doff;
    {
        const int row = lane >> 3;
        doff = (unsigned)(row * 1024 + nb * 128 + (((lane & 7) ^ (((row >> 1) & 1) << 2)) * 16));
    }
    const int row0 = 4 * h + q, sw = (row0 >> 1) & 1;
    const int wfo0 = row0 * 128 + sw * 64 + (p4 & 1) * 32 + blk * 16 + (p4 >> 1) * 8, wfo1 = wfo0 ^ 64;
    const char* hfa = ws + WS_H + ((ptrdiff_t)(2 * mb + mh) * NKF + fk0) * 1024;
    const char* hfb = ws + WS_H + ((ptrdiff_t)(2 * mb + 1 - mh) * NKF + fk0) * 1024;
    const char* wdp = wd + (ptrdiff_t)fk0 * 16384;
    {
        asm volatile("s_waitcnt vmcnt(11)" : "+v"(qf), "+v"(k0), "+v"(kk), "+v"(g0), "+v"(g1) :: "memory");
        STAMP(21);
        f32x16 z = {};
        f32x16 s = MFMA(k0, qf, z);
        bar();
        float m = fmaxf(s[0], s[1]);
#pragma unroll
        for (int i = 2; i < 16; i += 2) m = fmaxf(fmaxf(m, s[i]), s[i + 1]);
        m = fmaxf(m, __shfl_xor(m, 32));
        f32x16 negm;
#pragma unroll
        for (int i = 0; i < 16; ++i) negm[i] = -m;
        s = MFMA(k0, qf, negm);
        bf16x8 v0 = isv ? g0 : __builtin_bit_cast(bf16x8, ones), v1 = isv ? g1 : __builtin_bit_cast(bf16x8, ones);
        u32x4 p1 = {0, 0, 0, 0};
        bf16x8 v1p = v1;
        ldsptr krd = (ldsptr)smem + KOFF + lane * 16;
        ldsptr vrd0 = (ldsptr)smem + (isv ? voff : 1024);
        ldsptr vrd1 = (ldsptr)smem + (isv ? 512 + voff : 1024);
        ldsptr wr0 = (ldsptr)smem + wid * RING + wfo0, wr1 = (ldsptr)smem + wid * RING + wfo1;
        unsigned ringb = lds0 + wid * RING;
        SPIN(ringb);
        auto refill = [&](int e, const char* wsrc, int n, u64 mask) {
            glds16(wsrc, doff, ringb, e * 2048);
            glds16(wsrc + 8192, doff, ringb, e * 2048 + 1024);
            asm volatile("s_mov_b64 exec, %5\n\tglobal_load_dwordx4 %0, %2, %3 sc1\n\tglobal_load_dwordx4 %1, %2, %4 sc1\n\ts_mov_b64 exec, -1"
                         : "+v"(ha[e]), "+v"(hb[e]) : "v"(loff), "s"(hfa + n * 1024), "s"(hfb + n * 1024), "s"(mask) : "memory");
        };
        auto chunk = [&](auto fused_, int key, const char* ws0, int wst0, int wst, int n0, u64 m0_, u64 m1_, u64 m2_) {
            constexpr bool FU = decltype(fused_)::value;
            bf16x8 wf;
#pragma unroll
            for (int i = 0; i < 8; ++i) {
                const int e = i / 3, ph = i % 3;
                SBAR();
                if (!FU && i == 4) drop_if<(1 << 0)>(key);
                if (!FU && i == 6) drop_if<(1 << 10)>(key);
                if (!FU && i == 7) drop_if<(1 << 1) | (1 << 8)>(key);
                if (FU && i == FDO) drop_if<(1 << FCO)>(key);
                if (FU && i == FDY) drop_if<(1 << (8 + FCY))>(key);
                f32x16 sn = MFMA(kk, qf, negm);
                SBAR();
                kk = lds_b128(krd + i * 1024);
                bf16x8 nv0 = lds_b128(vrd0 + i * 1040);
                if (FU && ph == 0) wait_vm2<8>(ha[e], hb[e]);
                if (FU && ph < 2) wf = tr_frag((ph ? wr1 : wr0) + e * 2048, (ph ? wr1 : wr0) + e * 2048 + 1024);
                SBAR();
                u32x4 p0;
                { float a = EXP2(s[0]), b2 = EXP2(s[1]), c2 = EXP2(s[2]), d = EXP2(s[3]); p0[0] = cvtpk(a, b2); p0[1] = cvtpk(c2, d); }
                SBAR();
                o = MFMA(v1p, __builtin_bit_cast(bf16x8, p1), o);
                SBAR();
                v1p = v1;
                v1 = lds_b128(vrd1 + i * 1040);
                SBAR();
                { float a = EXP2(s[4]), b2 = EXP2(s[5]), c2 = EXP2(s[6]), d = EXP2(s[7]); p0[2] = cvtpk(a, b2); p0[3] = cvtpk(c2, d); }
                SBAR();
                float e8 = EXP2(s[8]), e9 = EXP2(s[9]);
                SBAR();
                o = MFMA(v0, __builtin_bit_cast(bf16x8, p0), o);
                SBAR();
                v0 = nv0;
                { float c2 = EXP2(s[10]), d = EXP2(s[11]); p1[0] = cvtpk(e8, e9); p1[1] = cvtpk(c2, d); }
                SBAR();
                if (FU && ph < 2) acc[ph] = MFMA(wf, ha[e], acc[ph]);
                SBAR();
                float e12 = EXP2(s[12]), e13 = EXP2(s[13]), e14 = EXP2(s[14]), e15 = EXP2(s[15]);
                SBAR();
                if (FU && ph < 2) acc[2 + ph] = MFMA(wf, hb[e], acc[2 + ph]);
                SBAR();
                p1[2] = cvtpk(e12, e13); p1[3] = cvtpk(e14, e15);
                SBAR();
                if (FU && (ph == 2 || i == 7)) refill(e, ws0 + (e == 0 ? wst0 : e == 1 ? 16384 : wst), n0 + e, e == 0 ? m0_ : e == 1 ? m1_ : m2_);
                SBAR();
                s = sn;
            }
            krd += CHK; vrd0 += CHK; vrd1 += CHK;
        };
        STAMP(22);
        issue_kv(4, 0); issue_kv(4, 1); issue_kv(5, 0); issue_kv(5, 1);
        if (wid == 7 && lane >= 48) *(LDSP u32x4*)((ldsptr)smem + (lane >> 3) * CHK + (lane & 7) * 1040 + 1024) = ones;
        if (LAD) __builtin_amdgcn_s_setprio(1);
#pragma unroll 1
        for (int c = 0;; ++c) {
            STAMP(30 + c);
            chunk(std::false_type{}, c + ch * 8, nullptr, 0, 0, 0, 0, 0, 0);
            if (c == 2) break;
            if (c == 0) {
                wait_vm<4>();
                bar();
                if (LAD) __builtin_amdgcn_s_setprio(1);
                flag_store(ws + WS_FH + rb * 128 + cg * 8, 0, tag, lead);
                issue_kv(6, 0); issue_kv(6, 1);
                if (wid < 7) issue_kv(7, 0);
                if (wid < 6) issue_kv(7, 1);
            } else {
#pragma unroll
                for (int e = 0; e < 4; ++e) glds16(wo + (wid * 4 + e) * 8192, doff, ldk * 4, WO_LO + e * 1024);
                if (wid == 0) glds4_sc1(ws + WS_FH + (mb >> 1) * 128, r * 4, lds0, POLL);
            }
        }
        wait_vm<0>();
        if (wid == 0) {
            const u64 v = *(LDSP u64*)((ldsptr)smem + POLL + (lane & 15) * 8);
            if (__builtin_expect(!__all((lane & 15) == 15 || v == tag), 0)) spin_flags(ws + WS_FH + (mb >> 1) * 128 + (lane & 15) * 8, (lane & 15) != 15, tag);
        }
        bar();
        if (LAD) __builtin_amdgcn_s_setprio(1);
#pragma unroll 1
        for (int c = 3; c < 8; ++c) {
            STAMP(30 + c);
            const int n0 = c < 7 ? 3 * c - 9 : 2;
            const char* w0 = c < 7 ? wdp + n0 * 16384 : wo + (16 + 2 * wid) * 16384;
            const u64 ml = c < 7 ? ~0ull : 1ull;
            chunk(std::true_type{}, c + ch * 8, w0, c == 3 ? 16384 : 0, c < 7 ? 32768 : 0, n0, c == 3 ? 0ull : ml, c == 3 && wid >= 6 ? 0ull : ml, ml);
        }
        o = MFMA(v1p, __builtin_bit_cast(bf16x8, p1), o);
        asm volatile("s_waitcnt vmcnt(0)" : "+v"(ha[0]), "+v"(hb[0]), "+v"(ha[1]), "+v"(hb[1]), "+v"(ha[2]), "+v"(hb[2]), "+v"(agate), "+v"(tgate) :: "memory");
    }
    STAMP(40);
    {
        float chk = 0.f;
#pragma unroll
        for (int i = 0; i < 8; ++i) chk += fabsf(o[i]) * 0.125f;
        const bool bad = !(o[8] <= 1.8446744e19f) || !(chk <= 3.0e38f);
        if (__builtin_expect(__any(bad), 0)) o = attn_fallback(kf, vf, qf, lane);
        const float rl = __builtin_amdgcn_rcpf(o[8] * (1.0f + EXP2(-LOG2E * (agate[0] + agate[1]))));
        gst16_sc1(ws + WS_O + ((size_t)AT * NH + head) * 1024, loff, pack8(o, 0, rl));
        wait_vm<0>();
        bar();
        if (tid < 8) __hip_atomic_store((gu64*)(ws + WS_FO + tid * 2048 + qb * 256) + head, tag, RLX_AGENT);
    }
    STAMP(41);
    LDSP f32x4* xch = (LDSP f32x4*)((ldsptr)smem + XCH);
#pragma unroll
    for (int t = 0; t < 2; ++t)
#pragma unroll
        for (int i = 0; i < 4; ++i) {
            f32x4 v = {acc[2 + t][4 * i], acc[2 + t][4 * i + 1], acc[2 + t][4 * i + 2], acc[2 + t][4 * i + 3]};
            xch[wid * 512 + (t * 4 + i) * 64 + lane] = v;
        }
    bf16x8 wa[8][2];
    {
        const int base = kq < 2 ? WO_LO + kq * 16384 : (kq - 2) * 4 * RING, st2 = kq < 2 ? 4096 : RING;
#pragma unroll
        for (int j = 0; j < 8; ++j) {
            ldsptr p = (ldsptr)smem + base + (j >> 1) * st2 + (j & 1) * 2048;
            wa[j][0] = tr_frag(p + wfo0, p + wfo0 + 1024);
            wa[j][1] = tr_frag(p + wfo1, p + wfo1 + 1024);
        }
    }
    if (wid == 0) spin_flags(ws + WS_FO + nb * 2048 + (mb >> 2) * 256 + r * 8, true, tag);
    if (__builtin_expect(wid == 1 && blockIdx.x == 0, 0)) {
#pragma unroll
        for (int i = 0; i < 4; ++i) spin_flags(ws + WS_FQ + i * 512 + lane * 8, true, tag);
        if (lane == 0) __hip_atomic_store((gu64*)(ws + WS_EP), tag, RLX_AGENT);
    }
    bar();
    STAMP(42);
    const char* of = ws + WS_O + ((size_t)(2 * mb + mh) * NH + kq * 8) * 1024;
    bf16x8 oa[8];
    const float og = 1.0f - __builtin_amdgcn_rcpf(1.0f + EXP2(-LOG2E * (tgate[0] + tgate[1])));
#pragma unroll
    for (int j = 0; j < 8; ++j) gld16_sc1(oa[j], of + j * 1024, loff);
#pragma unroll
    for (int t = 0; t < 2; ++t)
#pragma unroll
        for (int i = 0; i < 4; ++i) {
            const f32x4 v = xch[(wid ^ 1) * 512 + (t * 4 + i) * 64 + lane];
#pragma unroll
            for (int j = 0; j < 4; ++j) acc[t][4 * i + j] = (acc[t][4 * i + j] + v[j]) * og;
        }
#pragma unroll
    for (int j = 0; j < 8; ++j) {
        if (j == 0) wait_vm1<7>(oa[j]); else if (j == 1) wait_vm1<6>(oa[j]); else if (j == 2) wait_vm1<5>(oa[j]);
        else if (j == 3) wait_vm1<4>(oa[j]); else if (j == 4) wait_vm1<3>(oa[j]); else if (j == 5) wait_vm1<2>(oa[j]);
        else if (j == 6) wait_vm1<1>(oa[j]); else wait_vm1<0>(oa[j]);
        acc[0] = MFMA(wa[j][0], oa[j], acc[0]);
        acc[1] = MFMA(wa[j][1], oa[j], acc[1]);
        SBAR();
    }
    STAMP(43);
#pragma unroll
    for (int t = 0; t < 2; ++t)
#pragma unroll
        for (int i = 0; i < 4; ++i) {
            f32x4 v = {acc[t][4 * i], acc[t][4 * i + 1], acc[t][4 * i + 2], acc[t][4 * i + 3]};
            xch[(wid ^ 1) * 512 + (t * 4 + i) * 64 + lane] = v;
        }
    bar();
    f32x4 ra = {}, rb4 = {};
#pragma unroll
    for (int k = 0; k < 4; ++k) {
        ra += xch[((2 * k + mh) ^ 1) * 512 + (kq * 2) * 64 + lane];
        rb4 += xch[((2 * k + mh) ^ 1) * 512 + (kq * 2 + 1) * 64 + lane];
    }
    const u32x4 ov = {cvtpk(ra[0], ra[1]), cvtpk(ra[2], ra[3]), cvtpk(rb4[0], rb4[1]), cvtpk(rb4[2], rb4[3])};
    {
        const unsigned oo = (mb * 64 + mh * 32 + r) * 1024 + (nb * 64 + (kq >> 1) * 32 + h * 16 + (kq & 1) * 8) * 2;
        asm volatile("global_store_dwordx4 %0, %1, %2" :: "v"(oo), "v"(ov), "s"(out) : "memory");
    }
    if (CPF) asm volatile(".Lcode_end:");
}

extern "C" void kernel_launch(void* const* d_in, const int* in_sizes, int n_in, void* d_out, int out_size, void* d_ws,
                              size_t ws_size, hipStream_t stream) {
    (void)hipFuncSetAttribute((const void*)fused, hipFuncAttributeMaxDynamicSharedMemorySize, LDS_BYTES);
    fused<<<256, 512, LDS_BYTES, stream>>>((const u16*)d_in[0], (const u16*)d_in[1], (const u16*)d_in[2], (const u16*)d_in[4],
                                           (const u16*)d_in[5], (const u16*)d_in[6], (const u16*)d_in[7], (char*)d_ws + WSOFF, (u16*)d_out);
}
```

```cpp
#include <hip/hip_runtime.h>
#include <hip/hip_bf16.h>
#include <stdint.h>
#include <type_traits>

typedef unsigned short u16;
typedef unsigned long long u64;
typedef __attribute__((ext_vector_type(8))) __bf16 bf16x8;
typedef __attribute__((ext_vector_type(2))) __bf16 bf16x2;
typedef __attribute__((ext_vector_type(4))) short s16x4;
typedef __attribute__((ext_vector_type(16))) float f32x16;
typedef __attribute__((ext_vector_type(4))) float f32x4;
typedef __attribute__((ext_vector_type(2))) float f32x2;
typedef __attribute__((ext_vector_type(4))) unsigned u32x4;
#define LDSP __attribute__((address_space(3)))
typedef LDSP char* ldsptr;
typedef __attribute__((address_space(1))) unsigned gu32;
typedef __attribute__((address_space(1))) u64 gu64;

#define FPOS 8
#define G9 26
constexpr int S_ = 2048, F_ = 1376, NH = 32;
constexpr int NTT = S_ / 32;
constexpr int NKF = F_ / 16;
constexpr float LOG2E = 1.4426950408889634f;
constexpr float QSCALE = 0.25f * LOG2E;

constexpr size_t WS_EP = 0;
constexpr size_t WS_FQ = 1024;
constexpr size_t WS_FH = WS_FQ + 2048;
constexpr size_t WS_FO = WS_FH + 2048;
constexpr size_t WS_G = WS_FO + 16384;
constexpr size_t WS_Q = 262144;
constexpr size_t WS_K = WS_Q + (size_t)NH * NTT * 1024;
constexpr size_t WS_V = WS_K + (size_t)NH * NTT * 1024;
constexpr size_t WS_O = WS_V + (size_t)NH * NTT * 1024;
constexpr size_t WS_H = WS_O + (size_t)NTT * 32 * 1024;

constexpr size_t WS_DUMP = WS_H + (size_t)NTT * NKF * 1024;

constexpr int LDS_BYTES = 163840;
constexpr int SLOT = 28672;
__host__ __device__ constexpr int phys(int st) { return 20480 + (st < 8 && FPOS <= 4 ? (3 + (st & 3)) % 5 : (st + 4) % 5) * SLOT; }
constexpr int F1OFF = 4064, F1UP = 12320;
constexpr int VST = 1056, CHK = 16640, KOFF = 8 * VST;
constexpr int WO_LO = 131072, POLL = 7 * CHK + 7 * VST, XCH = 49536, RING = 6144;

#define MFMA(a, b, c) __builtin_amdgcn_mfma_f32_32x32x16_bf16((a), (b), (c), 0, 0, 0)
#define ROT 1
#define KROT 1
#define IBAR 4
#define NI 2
#define RATE 2
#define TPF 0
#define TPW 0
#define WSOFF 0
#define FLS 11
#define PB 22
#define POLLG (PB - 4)
#define EPB 2
#define SL0 2
#define SPIN(x) asm volatile("" : "+s"(x))
#define SBAR() __builtin_amdgcn_sched_barrier(0)
#define EXP2(x) __builtin_amdgcn_exp2f(x)
#define RLX_AGENT __ATOMIC_RELAXED, __HIP_MEMORY_SCOPE_AGENT
#define STAMP(i)
#define STAMPC(i)

__device__ __forceinline__ unsigned cvtpk(float lo, float hi) {
    f32x2 v = {lo, hi};
    return __builtin_bit_cast(unsigned, __builtin_convertvector(v, bf16x2));
}
__device__ __forceinline__ void glds16(const void* sbase, unsigned voff, unsigned lds_s, int lds_i) {
    asm volatile("s_add_u32 m0, %2, %3\n\ts_nop 0\n\tglobal_load_lds_dwordx4 %0, %1" :: "v"(voff), "s"(sbase), "s"(lds_s), "i"(lds_i) : "memory", "scc");
}
__device__ __forceinline__ void glds16k(unsigned blo, unsigned bhi, int k, int stride, unsigned voff, unsigned lds_s, int lds_i) {
    asm volatile("s_add_u32 m0, %1, %2\n\ts_mul_i32 vcc_lo, %3, %4\n\ts_add_u32 vcc_lo, vcc_lo, %5\n\ts_addc_u32 vcc_hi, %6, 0\n\tglobal_load_lds_dwordx4 %0, vcc"
                 :: "v"(voff), "s"(lds_s), "i"(lds_i), "s"(k), "i"(stride), "s"(blo), "s"(bhi) : "memory", "scc", "vcc");
}
__device__ __forceinline__ void glds16_sc1(const void* sbase, unsigned voff, unsigned lds_s, int lds_i) {
    asm volatile("s_add_u32 m0, %2, %3\n\ts_nop 0\n\tglobal_load_lds_dwordx4 %0, %1 sc1" :: "v"(voff), "s"(sbase), "s"(lds_s), "i"(lds_i) : "memory", "scc");
}
__device__ __forceinline__ void glds4_sc1(const void* sbase, unsigned voff, unsigned lds_s, int lds_i) {
    asm volatile("s_add_u32 m0, %2, %3\n\ts_nop 0\n\tglobal_load_lds_dword %0, %1 sc1" :: "v"(voff), "s"(sbase), "s"(lds_s), "i"(lds_i) : "memory", "scc");
}
__device__ __forceinline__ void gld16_sc1(bf16x8& r, const void* sbase, unsigned voff) {
    asm volatile("global_load_dwordx4 %0, %1, %2 sc1" : "=v"(r) : "v"(voff), "s"(sbase) : "memory");
}
__device__ __forceinline__ void gld16_sc1_tied(bf16x8& r, const void* sbase, unsigned voff) {
    asm volatile("global_load_dwordx4 %0, %1, %2 sc1" : "+v"(r) : "v"(voff), "s"(sbase) : "memory");
}
__device__ __forceinline__ void gld8_sc1(f32x2& r, const void* sbase, unsigned voff) {
    asm volatile("global_load_dwordx2 %0, %1, %2 sc1" : "=v"(r) : "v"(voff), "s"(sbase) : "memory");
}
__device__ __forceinline__ void gst16_sc1(void* sbase, unsigned voff, bf16x8 v) {
    asm volatile("global_store_dwordx4 %0, %1, %2 sc1\n\ts_nop 1" :: "v"(voff), "v"(v), "s"(sbase) : "memory");
}
__device__ __forceinline__ void gst4_sc1(void* sbase, unsigned voff, float v) {
    asm volatile("global_store_dword %0, %1, %2 sc1" :: "v"(voff), "v"(v), "s"(sbase) : "memory");
}
template <int N> __device__ __forceinline__ void wait_vm() { asm volatile("s_waitcnt vmcnt(%0)" ::"i"(N) : "memory"); }
template <int N> __device__ __forceinline__ void wait_vm1(bf16x8& a) { asm volatile("s_waitcnt vmcnt(%1)" : "+v"(a) : "i"(N) : "memory"); }
template <int N> __device__ __forceinline__ void wait_vm2(bf16x8& a, bf16x8& b) {
    asm volatile("s_waitcnt vmcnt(%2)" : "+v"(a), "+v"(b) : "i"(N) : "memory");
}
__device__ __forceinline__ void bar() { asm volatile("s_waitcnt lgkmcnt(0)\n\ts_barrier" ::: "memory"); }
__device__ __forceinline__ ldsptr ladd(ldsptr p, int off) {
    unsigned r;
    asm volatile("v_add_u32 %0, %1, %2" : "=v"(r) : "i"(off), "v"((unsigned)(uintptr_t)p));
    return (ldsptr)(uintptr_t)r;
}
__device__ __forceinline__ bf16x8 lds_b128(ldsptr p) { return *(LDSP bf16x8*)p; }
__device__ __forceinline__ bf16x8 tr_frag(ldsptr p0, ldsptr p1) {
    s16x4 a = __builtin_amdgcn_ds_read_tr16_b64_v4i16((LDSP s16x4*)p0), b = __builtin_amdgcn_ds_read_tr16_b64_v4i16((LDSP s16x4*)p1);
    typedef __attribute__((ext_vector_type(8))) short s16x8;
    s16x8 v = {a[0], a[1], a[2], a[3], b[0], b[1], b[2], b[3]};
    return __builtin_bit_cast(bf16x8, v);
}
__device__ __forceinline__ bf16x8 pack8(const f32x16& a, int o, float sc) {
    u32x4 v = {cvtpk(a[o] * sc, a[o + 1] * sc), cvtpk(a[o + 2] * sc, a[o + 3] * sc),
               cvtpk(a[o + 4] * sc, a[o + 5] * sc), cvtpk(a[o + 6] * sc, a[o + 7] * sc)};
    return __builtin_bit_cast(bf16x8, v);
}
__device__ __forceinline__ bf16x8 pack8(const f32x16& a, int o) {
    u32x4 v = {cvtpk(a[o], a[o + 1]), cvtpk(a[o + 2], a[o + 3]), cvtpk(a[o + 4], a[o + 5]), cvtpk(a[o + 6], a[o + 7])};
    return __builtin_bit_cast(bf16x8, v);
}
__device__ __forceinline__ void spin_flags(const char* p, bool need, u64 tag) {
#pragma unroll 1
    for (int spin = 0; spin < (1 << 20); ++spin) {
        const u64 v = __hip_atomic_load((gu64*)p, RLX_AGENT);
        if (__all(!need || v == tag)) break;
        __builtin_amdgcn_s_sleep(2);
    }
}

__host__ __device__ constexpr int item(int p) { return p < FPOS ? p : p == FPOS ? 8 : p <= 8 ? p - 1 : p; }
__host__ __device__ constexpr int pos(int s) { return s < FPOS ? s : s == 8 ? FPOS : s < 8 ? s + 1 : s; }
__host__ __device__ constexpr int endp(int p, int nq) {
    int n = 0;
    for (int i = 0; i <= p; ++i) n += item(i) < 8 ? nq : 3;
    return n;
}
__host__ __device__ constexpr int issued(int g, int nq) {
    int n = endp(NI - 1, nq);
    for (int i = 0; i < g; ++i) {
        const int b = (i + 2) >> 2, per = item(b + 4) < 8 ? nq : 3, op = ((i + 2) & 3) + 1;
        int t = b == 0 ? endp(4, nq) : endp(b + 3, nq) + (op < per ? op : per);
        const int cap = i < G9 ? endp(8, nq) : endp(15, nq);
        if (t > cap) t = cap;
        n = n + RATE < t ? n + RATE : t;
    }
    return n;
}
__host__ __device__ constexpr int posof(int n, int nq) {
    int p = 0;
    while (endp(p, nq) <= n) ++p;
    return p;
}
template <int NQ, int S, int G = (S == 0 ? 0 : 4 * S - 2)> __device__ __forceinline__ void wait_st() {
    constexpr int n = issued(G, NQ) - endp(pos(S), NQ) + (G >= 32 && endp(pos(S), NQ) <= issued(32, NQ) ? 3 : 0);
    static_assert(issued(G, NQ) >= endp(pos(S), NQ) && n < 64, "stage not issued");
    wait_vm<n>();
}
template <int I, int N, class F> __device__ __forceinline__ void sfor(F&& f) {
    if constexpr (I < N) { f(std::integral_constant<int, I>{}); sfor<I + 1, N>(f); }
}
static_assert(issued(32, 3) - endp(8, 3) == issued(32, 4) - endp(8, 4) && issued(32, 3) >= endp(8, 3), "common schedule from k-step 32 on");
__device__ __forceinline__ void flag_store(void* sbase, unsigned voff, u64 v, unsigned mask) {
    asm volatile("s_mov_b32 exec_lo, %3\n\ts_mov_b32 exec_hi, 0\n\tglobal_store_dwordx2 %0, %1, %2 sc1\n\ts_mov_b64 exec, -1" :: "v"(voff), "v"(v), "s"(sbase), "s"(mask) : "memory");
}
#define PIN(x) asm volatile("" : "+v"(x))
#define LAD 1
#define CPF 1
#define FDO 0
#define FCO 5
#define FDY 3
#define FCY 7
template <int MASK> __device__ __forceinline__ void drop_if(int key) {
    if (LAD) asm volatile("s_bitcmp0_b32 %1, %0\n\ts_cbranch_scc1 1f\n\ts_setprio 0\n1:" :: "s"(key), "i"(MASK) : "scc");
}

__device__ __forceinline__ f32x16 attn_fallback(const char* kf, const char* vf, bf16x8 qf, int lane) {
    f32x16 o = {};
    float m = -INFINITY;
    const bool isv = ((lane >> 4) & 1) == 0;
    const u32x4 ones = {0x3f803f80u, 0x3f803f80u, 0x3f803f80u, 0x3f803f80u};
    const unsigned voff = (lane >> 5) * 256 + (lane & 15) * 16;
#pragma unroll 1
    for (int t = 0; t < NTT; ++t) {
        bf16x8 kk, a0, a1;
        gld16_sc1(kk, kf + t * 1024, lane * 16);
        gld16_sc1(a0, vf + t * 1024, voff);
        gld16_sc1(a1, vf + t * 1024 + 512, voff);
        wait_vm1<0>(kk);
        wait_vm2<0>(a0, a1);
        u32x4 v0 = __builtin_bit_cast(u32x4, a0), v1 = __builtin_bit_cast(u32x4, a1);
        if (!isv) { v0 = ones; v1 = ones; }
        f32x16 z = {};
        f32x16 s = MFMA(kk, qf, z);
        float tm = s[0];
#pragma unroll
        for (int i = 1; i < 16; ++i) tm = fmaxf(tm, s[i]);
        tm = fmaxf(tm, __shfl_xor(tm, 32));
        const float mn = fmaxf(m, tm);
        const float al = EXP2(m - mn);
        m = mn;
#pragma unroll
        for (int i = 0; i < 16; ++i) { o[i] *= al; s[i] = EXP2(s[i] - mn); }
        o = MFMA(__builtin_bit_cast(bf16x8, v0), pack8(s, 0), o);
        o = MFMA(__builtin_bit_cast(bf16x8, v1), pack8(s, 8), o);
    }
    return o;
}

__global__ __launch_bounds__(512, 2) void fused(const u16* __restrict__ x_, const u16* __restrict__ wqkv_, const u16* __restrict__ wo_,
                                                const u16* __restrict__ wg_, const u16* __restrict__ wu_, const u16* __restrict__ wd_,
                                                const u16* __restrict__ gatew_, char* __restrict__ ws_a, u16* __restrict__ out_) {
    extern __shared__ __attribute__((aligned(16))) char smem[];
    STAMP(0); STAMPC(61);
    typedef __attribute__((ext_vector_type(8))) u64 u64x8;
    u64x8 ka;
    u64 kb;
    asm volatile("s_load_dwordx16 %0, %2, 0\n\ts_load_dwordx2 %1, %2, 0x40" : "=&s"(ka), "=&s"(kb) : "s"(__builtin_amdgcn_kernarg_segment_ptr()));
    const int tid = threadIdx.x, lane = tid & 63;
    const int wid = __builtin_amdgcn_readfirstlane(tid >> 6);
    const int xcd = blockIdx.x & 7, idx = blockIdx.x >> 3;
    const unsigned lds0 = 0;
    const unsigned loff = lane * 16;
    if (CPF) {
        u64 pc;
        unsigned len;
        asm volatile("s_getpc_b64 %0\n.Lcode_begin:\n\ts_mov_b32 %1, .Lcode_end-.Lcode_begin-4" : "=s"(pc), "=s"(len));
        const unsigned line = min((unsigned)(((idx % 3) * 128 + wid * 16 + (lane & 15)) * 128), len);
        asm volatile("s_mov_b32 m0, %2\n\ts_nop 0\n\tglobal_load_lds_dword %0, %1" :: "v"(line), "s"(pc), "i"(4096) : "memory");
    }
    const int r = lane & 31, h = lane >> 5, q = (lane & 15) >> 2, p4 = lane & 3, blk = (lane >> 4) & 1;
    const int rb = (xcd & 1) * 8 + (idx & 7), cg = (xcd >> 1) * 4 + (idx >> 3);
    const int tt = wid & 3, ch = wid >> 2, hg = cg >> 1, odd = cg & 1;
    const int rot = ROT * ((idx & 7) + 2 * (idx >> 3));
    unsigned xoff[2], woff[2], foff[3];
#pragma unroll
    for (int e = 0; e < 2; ++e) {
        const int row = wid * 16 + e * 8 + (lane >> 3);
        xoff[e] = (unsigned)((rb * 128 + row) * 1024 + (((lane & 7) ^ ((row >> 1) & 7) ^ (odd * 4)) * 16));
    }
    const int wp0 = ch ? 2 * wid - 4 : wid;
#pragma unroll
    for (int e = 0; e < 2; ++e) {
        const int n = (wp0 + e) * 64 + lane, kr = n / 12, c = n % 12;
        woff[e] = (unsigned)((kr ^ (odd * 32)) * 3072 + hg * 128 + (c < 8 ? odd * 2048 + c * 16 : 1024 - 128 + odd * 64 + c * 16));
    }
    unsigned ldx = lds0 + wid * 2048, ldw = lds0 + wp0 * 1024;
    unsigned fdst = lds0 + F1OFF + ch * F1UP + tt * 3072;
    SPIN(ldx); SPIN(ldw); SPIN(fdst);
    int kbs[8];
#pragma unroll
    for (int i = 0; i < 8; ++i) { kbs[i] = (i + rot) & 7; SPIN(kbs[i]); }
    const int u0 = cg < 14 ? cg * 6 : 80;
#pragma unroll
    for (int e = 0; e < 3; ++e) {
        const int n = (tt * 3 + e) * 64 + lane, kr = n / 12, c = n % 12;
        foff[e] = (unsigned)((kr ^ (odd * 32)) * (F_ * 2) + u0 * 32 + c * 16);
    }
    unsigned goff = ((loff ^ (odd * 64)) + rot * 128) & 1023;
    PIN(xoff[0]); PIN(xoff[1]); PIN(woff[0]); PIN(woff[1]); PIN(foff[0]); PIN(foff[1]); PIN(foff[2]); PIN(goff);
    asm volatile("s_waitcnt lgkmcnt(0)" : "+s"(ka), "+s"(kb));
    STAMP(59);
    const char* wo = (const char*)ka[2];
    const char* wd = (const char*)ka[5];
    char* ws = (char*)ka[7];
    char* out = (char*)kb;
    u64 ep;
    asm volatile("s_load_dwordx2 %0, %1, 0" : "=&s"(ep) : "s"(ws + WS_EP));
    if (TPF && wid == TPW) {
        const int a = idx & 7;
        const u64 base = a == 0 ? ka[0] : a == 1 ? ka[1] : a == 2 ? ka[2] : a == 3 ? ka[3] : a == 4 ? ka[4] : a == 5 ? ka[5] : a == 6 ? kb : ka[7];
        const unsigned step = a == 7 ? (1u << 20) : a == 2 ? (1u << 15) : a == 1 ? 98304u : (a == 0 || a == 6) ? (1u << 17) : 88064u;
        const unsigned off = ((idx >> 3) * 4 + (lane & (TPF - 1)) * (4 / TPF)) * step;
        asm volatile("s_mov_b32 m0, %2\n\ts_nop 0\n\tglobal_load_lds_dword %0, %1" :: "v"(off), "s"(base), "i"(4096) : "memory");
    }
    const u64 fsrc = ch ? ka[4] : ka[3];
    unsigned xlo = ka[0], xhi = ka[0] >> 32, wlo = ka[1], whi = ka[1] >> 32, flo = fsrc, fhi = fsrc >> 32;
    SPIN(xlo); SPIN(xhi); SPIN(wlo); SPIN(whi); SPIN(flo); SPIN(fhi);
    auto issue_q = [&](int st, int op) {
        if (op == 0) glds16k(xlo, xhi, kbs[st], 128, xoff[0], ldx, phys(st));
        if (op == 1) glds16k(wlo, whi, kbs[st], 64 * 3072, woff[0], ldw, phys(st) + 16384);
        if (op == 2) glds16k(xlo, xhi, kbs[st], 128, xoff[1], ldx, phys(st) + 1024);
        if (op == 3) glds16k(wlo, whi, kbs[st], 64 * 3072, woff[1], ldw, phys(st) + 16384 + 1024);
    };
    auto issue_f = [&](int st, int op) { glds16k(flo, fhi, kbs[st & 7], 64 * F_ * 2, foff[op], fdst, phys(st) + op * 1024); };
    if (wid == 3) glds16((const char*)ka[6], goff, lds0, 0);
#pragma unroll
    for (int st = 0; st < NI; ++st) {
        if (item(st) == 8) { issue_f(8, 0); issue_f(8, 1); issue_f(8, 2); }
        else {
            issue_q(item(st), 0); issue_q(item(st), 1); issue_q(item(st), 2);
            if (ch) issue_q(item(st), 3);
        }
        if (st < IBAR && st < NI - 1) asm volatile("s_barrier" ::: "memory");
    }
    STAMP(1);
    asm volatile("s_waitcnt lgkmcnt(0)" : "+s"(ep));
    const u64 tag = (ep ^ (ep >> 31)) * 0x9E3779B97F4A7C15ull + 0xD1B54A32D192ED03ull;
    unsigned lead = __builtin_amdgcn_readfirstlane(tid < 64 ? 1 : 0);
    SPIN(lead);
    bf16x8 xr[32];
    const int T = rb * 4 + tt;
    bf16x8 fw[3][3];
    ldsptr frd = (ldsptr)smem + F1OFF + (8 * h + q) * 192 + blk * F1UP + p4 * 8 + ch * 96, fs_;
    auto frd_ = [&](int g) {
        if ((g & 3) == 0) fs_ = ladd(frd, phys(8 + (g >> 2)));
        const int so = (g & 3) * 3072;
#pragma unroll
        for (int u = 0; u < 3; ++u) fw[g % 3][u] = tr_frag(fs_ + so + u * 32, fs_ + so + u * 32 + 768);
    };
    {
        ldsptr xrd[4];
#pragma unroll
        for (int ks = 0; ks < 4; ++ks) xrd[ks] = (ldsptr)smem + (tt * 32 + r) * 128 + (((2 * ks + h) ^ ((r >> 1) & 7)) * 16);
        ldsptr wrd = (ldsptr)smem + 16384 + (8 * h + q) * 192 + blk * 32 + p4 * 8 + ch * 128;
        ldsptr grd = (ldsptr)smem + h * 16;
        auto qkv = [&](auto var) {
            constexpr int VAR = decltype(var)::value;
            constexpr int NQ = VAR == 2 ? 4 : 3;
            f32x16 a0 = {}, a1 = {};
            bf16x8 w0[3], w1[3];
            ldsptr ws_;
            auto rd = [&](int g) {
                const int so = phys(g >> 2), ks = g & 3;
                if (ks == 0) ws_ = ladd(wrd, so);
                xr[g] = lds_b128(ladd(xrd[ks], so));
                w0[g % 3] = tr_frag(ws_ + ks * 3072, ws_ + ks * 3072 + 768);
                if (VAR != 2) w1[g % 3] = tr_frag(ws_ + ks * 3072 + 64, ws_ + ks * 3072 + 64 + 768);
                else if (ks < 2) w1[g % 3] = lds_b128(grd + g * 32);
            };
            wait_st<NQ, 0>();
            bar();
            STAMP(2);
            rd(0);
            rd(1);
            sfor<0, 32>([&](auto g_) {
                constexpr int g = decltype(g_)::value, s = (g + 2) >> 2, op = (g + 2) & 3;
                if constexpr (op == 0) {
                    wait_st<NQ, s>();
                    bar();
                    STAMP(2 + s);
                }
                if (g < 30) rd(g + 2); else frd_(g - 30);
                SBAR();
                if (VAR == 1) { a0 = MFMA(xr[g], w0[g % 3], a0); a1 = MFMA(xr[g], w1[g % 3], a1); }
                else { a0 = MFMA(w0[g % 3], xr[g], a0); if (VAR == 0 || (g & 3) < 2) a1 = MFMA(w1[g % 3], xr[g], a1); }
                PIN(a0); PIN(a1);
                SBAR();
                sfor<issued(g, NQ), issued(g + 1, NQ)>([&](auto n_) {
                    constexpr int n = decltype(n_)::value, ps = posof(n, NQ), st = item(ps), o_ = n - endp(ps - 1, NQ);
                    if (st < 8) issue_q(st, o_); else issue_f(st, o_);
                });
                SBAR();
            });
            STAMP(11);
            if (VAR == 0) {
                char* qp = ws + WS_Q + ((size_t)(4 * hg) * NTT + T) * 1024;
                gst16_sc1(qp, loff, pack8(a0, 0, QSCALE));
                gst16_sc1(qp + NTT * 1024, loff, pack8(a0, 8, QSCALE));
                gst16_sc1(qp + 2 * NTT * 1024, loff, pack8(a1, 0, QSCALE));
                gst16_sc1(qp + 3 * NTT * 1024, loff, pack8(a1, 8, QSCALE));
            } else if (VAR == 1) {
                char* vp = ws + WS_V + ((size_t)(4 * hg) * NTT + T) * 1024;
                const unsigned vo = blk * (NTT * 1024) + h * 256 + (lane & 15) * 16;
                gst16_sc1(vp, vo, pack8(a0, 0));
                gst16_sc1(vp + 512, vo, pack8(a0, 8));
                gst16_sc1(vp + 2 * NTT * 1024, vo, pack8(a1, 0));
                gst16_sc1(vp + 2 * NTT * 1024 + 512, vo, pack8(a1, 8));
            } else {
                char* kp = ws + WS_K + ((size_t)(4 * hg + 2 * odd) * NTT + T) * 1024;
                gst16_sc1(kp, loff, pack8(a0, 0));
                gst16_sc1(kp + NTT * 1024, loff, pack8(a0, 8));
                gst4_sc1(ws + WS_G + (hg * S_ + T * 32) * 8 + odd * 4, r * 8, a1[0]);
            }
        };
        if (ch) qkv(std::integral_constant<int, 2>{});
        else if (odd) qkv(std::integral_constant<int, 1>{});
        else qkv(std::integral_constant<int, 0>{});
    }
    const int head = xcd * 4 + (idx & 3), qb = idx >> 2;
    const int AT = qb * 8 + wid;
    const int krot = KROT * 8 * qb;
    const char* kf = ws + WS_K + (size_t)head * NTT * 1024;
    const char* vf = ws + WS_V + (size_t)head * NTT * 1024;
    const bool isv = blk == 0;
    const unsigned voff = h * 256 + (lane & 15) * 16;
    const int mb = xcd * 4 + (idx & 3), nb = idx >> 2;
    const int mh = wid & 1, kq = wid >> 1;
    bf16x8 qf, k0, kk, g0, g1;
    f32x2 agate, tgate;
    unsigned ldv = lds0 + wid * VST, ldk = lds0 + wid * 1024;
    SPIN(ldv); SPIN(ldk);
    auto issue_kv = [&](int c, int op) {
        if (op == 0) glds16_sc1(vf + ((8 * c + 1 + wid + krot) & 63) * 1024, loff, ldv, c * CHK);
        else glds16_sc1(kf + ((8 * c + 2 + wid + krot) & 63) * 1024, loff, ldk, c * CHK + KOFF);
    };
    f32x16 o, acc[4];
    bf16x8 ha[3], hb[3];
    const int fk0 = wid * 11 - (wid > 6 ? 1 : 0) - (wid < 6 ? 1 : 2);
    unsigned doff, cst;
    ldsptr krd, vrd0, vrd1, wr0, wr1;
    char* hp[3];
    f32x16 fa[3] = {};
    const int row0 = 4 * h + q, sw = (row0 >> 1) & 1;
    const int wfo0 = row0 * 128 + sw * 64 + (p4 & 1) * 32 + blk * 16 + (p4 >> 1) * 8, wfo1 = wfo0 ^ 64;
    const char* hfa = ws + WS_H + ((ptrdiff_t)(2 * mb + mh) * NKF + fk0) * 1024;
    const char* hfb = ws + WS_H + ((ptrdiff_t)(2 * mb + 1 - mh) * NKF + fk0) * 1024;
    const char* wdp = wd + (ptrdiff_t)fk0 * 16384;
    {
        u64 pollv = 0;
        const char* fq = ws + WS_FQ + xcd * 256;
        STAMP(12);
        sfor<0, 32>([&](auto g_) {
            constexpr int g = decltype(g_)::value, s = 8 + ((g + 2) >> 2), op = (g + 2) & 3;
            if constexpr (op == 0 && s <= 13) {
                if constexpr (s == FLS) wait_vm<issued(32 + g, 3) - issued(32, 3)>();
                if constexpr (s == 13) wait_st<3, 15, 32 + g>(); else wait_st<3, s>();
                STAMP(35 + s);
                bar();
                STAMP(4 + s);
                if (s == FLS) flag_store(ws + WS_FQ + hg * 256 + (odd * 16 + rb) * 8, 0, tag, lead);
            }
            if (g == PB) {
                asm volatile("s_waitcnt vmcnt(0)" : "+v"(pollv) :: "memory");
                if (__builtin_expect(wid == 0 && !__all(pollv == tag), 0)) spin_flags(fq + r * 8, true, tag);
                STAMP(49);
                bar();
                STAMP(18);
            }
            if (g < 30) frd_(g + 2);
            SBAR();
#pragma unroll
            for (int u = 0; u < 3; ++u) fa[u] = MFMA(fw[g % 3][u], xr[g], fa[u]);
            PIN(fa[0]); PIN(fa[1]); PIN(fa[2]);
            SBAR();
            sfor<issued(32 + g, 3), issued(33 + g, 3)>([&](auto n_) { constexpr int n = decltype(n_)::value; issue_f(n / 3, n % 3); });
            if (g == POLLG) asm volatile("global_load_dwordx2 %0, %1, %2 sc1" : "+v"(pollv) : "v"(r * 8), "s"(fq) : "memory");
            if (g == PB) { gld16_sc1(qf, ws + WS_Q + ((size_t)head * NTT + AT) * 1024, loff); gld16_sc1(k0, kf + krot * 1024, loff); }
            if (g == PB + 1) { issue_kv(0, 0); issue_kv(0, 1); }
            if (g == PB + 2) { gld16_sc1(kk, kf + (krot + 1) * 1024, loff); gld16_sc1(g0, vf + krot * 1024, voff); }
            if (g == PB + 3) { gld16_sc1(g1, vf + krot * 1024 + 512, voff); gld8_sc1(agate, ws + WS_G + (xcd * S_ + AT * 32) * 8, r * 8); }
            if (g == PB + 4) gld8_sc1(tgate, ws + WS_G + (xcd * S_ + (2 * mb + mh) * 32) * 8, r * 8);
#pragma unroll
            for (int n = 0; n < 6; ++n)
                if (g == PB + 4 + n * (28 - PB) / 6) issue_kv(1 + n / 2, n & 1);
            if (g == PB) {
                const unsigned v = lane & 8 ? 0u : 0x3f803f80u;
                *(LDSP u32x4*)((ldsptr)smem + min(wid, 6) * CHK + (lane & 7) * VST + 1024 + (lane & 8) * 2) = u32x4{v, v, v, v};
            }
            if (g == 16) {
                const int row = lane >> 3;
                doff = (unsigned)(row * 1024 + nb * 128 + (((lane & 7) ^ (((row >> 1) & 1) << 2)) * 16));
                wr0 = (ldsptr)smem + wid * RING + wfo0; wr1 = (ldsptr)smem + wid * RING + wfo1;
                PIN(doff); PIN(wr0); PIN(wr1);
            }
            if (g == 17) {
                const bool one = (lane & 11) == 0;
                cst = one ? 0x3f803f80u : 0u;
                krd = (ldsptr)smem + KOFF + lane * 16;
                vrd0 = (ldsptr)smem + (isv ? voff : one ? 1024 : 1040);
                vrd1 = (ldsptr)smem + (isv ? 512 + voff : one ? 1024 : 1040);
                PIN(cst); PIN(krd); PIN(vrd0); PIN(vrd1);
            }
            if (g == 18) {
#pragma unroll
                for (int u = 0; u < 3; ++u) {
                    const bool own = (cg < 14) | ((cg == 14) & (ch * 3 + u >= 4));
                    unsigned a = WS_H + (T * NKF + u0 + ch * 3 + u) * 1024, b = WS_DUMP + (blockIdx.x * 8 + wid) * 1024;
                    SPIN(a); SPIN(b);
                    hp[u] = ws + (own ? a : b);
                    SPIN(hp[u]);
                }
            }
            if (g >= 24 && g < 28) { acc[g - 24] = f32x16{}; PIN(acc[g - 24]); }
            if (g == 28) { o = f32x16{}; PIN(o); }
            if (g >= 29) { ha[g - 29] = bf16x8{}; hb[g - 29] = bf16x8{}; PIN(ha[g - 29]); PIN(hb[g - 29]); }
            SBAR();
        });
    }
    STAMP(20);
    {
        asm volatile("s_waitcnt vmcnt(13)" : "+v"(qf), "+v"(k0) :: "memory");
        f32x16 z = {};
        f32x16 s = MFMA(k0, qf, z);
        SBAR();
        if (ch) {
            wait_vm<11>();
            bar();
            if (LAD) __builtin_amdgcn_s_setprio(1);
            const unsigned v = lane & 8 ? 0u : 0x3f803f80u;
            *(LDSP u32x4*)((ldsptr)smem + 7 * CHK + (lane & 7) * VST + 1024 + (lane & 8) * 2) = u32x4{v, v, v, v};
        }
        STAMP(21);
        auto silu = [&](int u) {
            float e[8];
            f32x16 t;
            SBAR();
#pragma unroll
            for (int j = 0; j < 8; ++j) e[j] = -LOG2E * fa[u][j];
            SBAR();
#pragma unroll
            for (int j = 0; j < 8; ++j) e[j] = EXP2(e[j]);
            SBAR();
#pragma unroll
            for (int j = 0; j < 8; ++j) t[j] = fa[u][j] * fa[u][8 + j];
#pragma unroll
            for (int j = 0; j < 8; ++j) e[j] += 1.0f;
            SBAR();
#pragma unroll
            for (int j = 0; j < 8; ++j) e[j] = __builtin_amdgcn_rcpf(e[j]);
            SBAR();
#pragma unroll
            for (int j = 0; j < 8; ++j) t[j] *= e[j];
            SBAR();
            bf16x8 hv = pack8(t, 0);
            PIN(hv);
            SBAR();
            return hv;
        };
        const bf16x8 h0 = silu(0);
        float m = fmaxf(s[0], s[1]);
#pragma unroll
        for (int i = 2; i < 16; i += 2) m = fmaxf(fmaxf(m, s[i]), s[i + 1]);
        {
            float m2 = m;
            asm volatile("s_nop 1\n\tv_permlane32_swap_b32 %0, %1\n\ts_nop 1" : "+v"(m), "+v"(m2));
            m = fmaxf(m, m2);
        }
        f32x16 negm;
#pragma unroll
        for (int i = 0; i < 16; ++i) negm[i] = -m;
        const bf16x8 h1 = silu(1);
        if (!ch) {
            wait_vm<11>();
            bar();
        }
        gst16_sc1(hp[0], loff, h0);
        s = MFMA(k0, qf, negm);
        gst16_sc1(hp[1], loff, h1);
        gst16_sc1(hp[2], loff, silu(2));
        asm volatile("s_waitcnt vmcnt(11)" : "+v"(kk), "+v"(g0), "+v"(g1) :: "memory");
        const u32x4 cs4 = {cst, cst, cst, cst};
        bf16x8 v0 = isv ? g0 : __builtin_bit_cast(bf16x8, cs4), v1 = isv ? g1 : __builtin_bit_cast(bf16x8, cs4);
        u32x4 p1 = {0, 0, 0, 0};
        bf16x8 v1p = v1;
        unsigned ringb = lds0 + wid * RING;
        SPIN(ringb);
        auto refill = [&](int e, const char* wsrc, int n, u64 mask) {
            glds16(wsrc, doff, ringb, e * 2048);
            glds16(wsrc + 8192, doff, ringb, e * 2048 + 1024);
            asm volatile("s_mov_b64 exec, %5\n\tglobal_load_dwordx4 %0, %2, %3 sc1\n\tglobal_load_dwordx4 %1, %2, %4 sc1\n\ts_mov_b64 exec, -1"
                         : "+v"(ha[e]), "+v"(hb[e]) : "v"(loff), "s"(hfa + n * 1024), "s"(hfb + n * 1024), "s"(mask) : "memory");
        };
        const char* sp0 = vf;
        const char* sp1 = kf;
        unsigned soff = ((33 + wid + krot) & 63) * 1024, smsk = 0xffff, si0 = 1024, si1 = 7168;
        unsigned sd0 = wid * VST + 4 * CHK, sd1 = wid * 1024 + KOFF + 4 * CHK, sdi = CHK;
        const int w6 = wid < 6 ? 3 : 4;
        auto slot = [&](int c, int j, unsigned vo) {
            asm volatile("s_mov_b32 m0, %1\n\ts_add_u32 vcc_lo, %3, %0\n\ts_addc_u32 vcc_hi, %4, 0\n\tglobal_load_lds_dwordx4 %2, vcc sc1\n\t"
                         "s_add_u32 %0, %0, %5\n\ts_and_b32 %0, %0, %6\n\ts_add_u32 %1, %1, %7"
                         : "+s"(soff), "+s"((j & 1) ? sd1 : sd0)
                         : "v"(vo), "s"((unsigned)(u64)((j & 1) ? sp1 : sp0)), "s"((unsigned)((u64)((j & 1) ? sp1 : sp0) >> 32)), "s"((j & 1) ? si1 : si0), "s"(smsk), "s"(sdi)
                         : "memory", "scc", "vcc");
            asm volatile("s_cmp_eq_u32 %3, %4\n\ts_cselect_b64 exec, -1, 0\n\tglobal_load_dwordx4 %0, %1, %2 sc1\n\ts_mov_b64 exec, -1"
                         : "+v"((j & 1) ? hb[1 + (j >> 1)] : ha[1 + (j >> 1)]) : "v"(loff), "s"(((j & 1) ? hfb : hfa) + (1 + (j >> 1)) * 1024), "s"(c), "s"(j < 2 ? w6 : 3) : "memory", "scc");
        };
        auto chunk = [&](auto fused_, int c, int key, const char* ws0, int wst, int n0, u64 ml) {
            constexpr bool FU = decltype(fused_)::value;
            bf16x8 wf;
            const unsigned vo = c < 2 ? loff : doff;
#pragma unroll
            for (int i = 0; i < 8; ++i) {
                const int e = i / 3, ph = i % 3;
                SBAR();
                if (!FU && i == 6) drop_if<(1 << 10)>(key);
                if (!FU && i == 7) drop_if<(1 << 1) | (1 << 8)>(key);
                if (FU && i == FDO) drop_if<(1 << FCO)>(key);
                if (FU && i == FDY) drop_if<(1 << (8 + FCY))>(key);
                f32x16 sn = MFMA(kk, qf, negm);
                SBAR();
                kk = lds_b128(krd + i * 1024);
                bf16x8 nv0 = lds_b128(vrd0 + i * VST);
                if (FU && ph == 0) wait_vm2<8>(ha[e], hb[e]);
                if (FU && ph < 2) wf = tr_frag((ph ? wr1 : wr0) + e * 2048, (ph ? wr1 : wr0) + e * 2048 + 1024);
                SBAR();
                u32x4 p0;
                { float a = EXP2(s[0]), b2 = EXP2(s[1]), c2 = EXP2(s[2]), d = EXP2(s[3]); p0[0] = cvtpk(a, b2); p0[1] = cvtpk(c2, d); }
                SBAR();
                o = MFMA(v1p, __builtin_bit_cast(bf16x8, p1), o);
                SBAR();
                v1p = v1;
                v1 = lds_b128(vrd1 + i * VST);
                SBAR();
                { float a = EXP2(s[4]), b2 = EXP2(s[5]), c2 = EXP2(s[6]), d = EXP2(s[7]); p0[2] = cvtpk(a, b2); p0[3] = cvtpk(c2, d); }
                SBAR();
                float e8 = EXP2(s[8]), e9 = EXP2(s[9]);
                SBAR();
                o = MFMA(v0, __builtin_bit_cast(bf16x8, p0), o);
                SBAR();
                v0 = nv0;
                { float c2 = EXP2(s[10]), d = EXP2(s[11]); p1[0] = cvtpk(e8, e9); p1[1] = cvtpk(c2, d); }
                SBAR();
                if (FU && ph < 2) acc[ph] = MFMA(wf, ha[e], acc[ph]);
                SBAR();
                float e12 = EXP2(s[12]), e13 = EXP2(s[13]), e14 = EXP2(s[14]), e15 = EXP2(s[15]);
                SBAR();
                if (FU && ph < 2) acc[2 + ph] = MFMA(wf, hb[e], acc[2 + ph]);
                SBAR();
                p1[2] = cvtpk(e12, e13); p1[3] = cvtpk(e14, e15);
                SBAR();
                if (FU && (ph == 2 || i == 7)) refill(e, ws0 + (e == 0 ? 0 : e == 1 ? 16384 : wst), n0 + e, ml);
                if (!FU && i >= SL0 && i < SL0 + 4) slot(c, i - SL0, vo);
                SBAR();
                s = sn;
            }
            krd += CHK; vrd0 += CHK; vrd1 += CHK;
        };
        STAMP(22);
#pragma unroll 1
        for (int c = 0;; ++c) {
            STAMP(30 + c);
            chunk(std::false_type{}, c, c + ch * 8, nullptr, 0, 0, 0);
            if (c == 3) break;
            if (c != 1) {
                wait_vm<8>();
                if (c + wid * 4 == 30) {
                    const u64 v = *(LDSP u64*)((ldsptr)smem + POLL + (lane & 15) * 8);
                    if (__builtin_expect(!__all((lane & 15) == 15 || v == tag), 0)) spin_flags(ws + WS_FH + (mb >> 1) * 128 + (lane & 15) * 8, (lane & 15) != 15, tag);
                }
                STAMP(51 + c / 2);
                bar();
                if (LAD) __builtin_amdgcn_s_setprio(1);
                flag_store(ws + WS_FH + rb * 128 + cg * 8, 0, tag, c == 0 ? lead : 0);
            } else if (wid == 7) glds4_sc1(ws + WS_FH + (mb >> 1) * 128, r * 4, lds0, POLL);
            if (c) {
                sp0 = sp1 = c == 1 ? wo + ((wid + 2) & 7) * 8192 : wdp + 16384;
                soff = 0; smsk = ~0u;
                si0 = si1 = c == 1 ? 65536 : 8192;
                sd0 = c == 1 ? WO_LO + ((wid + 2) & 7) * 1024 : wid * RING + 2048;
                sd1 = sd0 + (c == 1 ? 8192 : 1024);
                sdi = c == 1 ? 16384 : 2048;
            }
        }
#pragma unroll 1
        for (int c = 4; c < 8; ++c) {
            STAMP(30 + c);
            const int n0 = c < 7 ? 3 * c - 9 : 2;
            const char* w0 = c < 7 ? wdp + n0 * 16384 : wo + (16 + 2 * wid) * 16384;
            chunk(std::true_type{}, c, c + ch * 8, w0, c < 7 ? 32768 : 0, n0, c < 7 ? ~0ull : 1ull);
        }
        o = MFMA(v1p, __builtin_bit_cast(bf16x8, p1), o);
        asm volatile("s_waitcnt vmcnt(0)" : "+v"(ha[0]), "+v"(hb[0]), "+v"(ha[1]), "+v"(hb[1]), "+v"(ha[2]), "+v"(hb[2]), "+v"(agate), "+v"(tgate) :: "memory");
    }
    STAMP(40);
    {
        float chk = 0.f;
#pragma unroll
        for (int i = 0; i < 8; ++i) chk += fabsf(o[i]) * 0.125f;
        const bool bad = !(o[8] <= 1.8446744e19f) || !(chk <= 3.0e38f);
        if (__builtin_expect(__any(bad), 0)) o = attn_fallback(kf, vf, qf, lane);
        const float rl = __builtin_amdgcn_rcpf(o[8] * (1.0f + EXP2(-LOG2E * (agate[0] + agate[1]))));
        gst16_sc1(ws + WS_O + ((size_t)AT * NH + head) * 1024, loff, pack8(o, 0, rl));
        wait_vm<0>();
        STAMP(53);
        bar();
        if (tid < 8) __hip_atomic_store((gu64*)(ws + WS_FO + tid * 2048 + qb * 256) + head, tag, RLX_AGENT);
    }
    STAMP(41);
    LDSP f32x4* xch = (LDSP f32x4*)((ldsptr)smem + XCH);
#pragma unroll
    for (int t = 0; t < 2; ++t)
#pragma unroll
        for (int i = 0; i < 4; ++i) {
            f32x4 v = {acc[2 + t][4 * i], acc[2 + t][4 * i + 1], acc[2 + t][4 * i + 2], acc[2 + t][4 * i + 3]};
            xch[wid * 512 + (t * 4 + i) * 64 + lane] = v;
        }
    bf16x8 wa[8][2];
    {
        const int base = kq < 2 ? WO_LO + kq * 16384 : (kq - 2) * 4 * RING, st2 = kq < 2 ? 4096 : RING;
#pragma unroll
        for (int j = 0; j < 8; ++j) {
            ldsptr p = (ldsptr)smem + base + (j >> 1) * st2 + (j & 1) * 2048;
            wa[j][0] = tr_frag(p + wfo0, p + wfo0 + 1024);
            wa[j][1] = tr_frag(p + wfo1, p + wfo1 + 1024);
        }
    }
    if (wid == 0) {
        spin_flags(ws + WS_FO + nb * 2048 + (mb >> 2) * 256 + r * 8, true, tag);
        if (blockIdx.x == 0 && lane == 0) __hip_atomic_store((gu64*)(ws + WS_EP), tag, RLX_AGENT);
    }
    STAMP(54);
    bar();
    STAMP(42);
    const char* of = ws + WS_O + ((size_t)(2 * mb + mh) * NH + kq * 8) * 1024;
    bf16x8 oa[8];
    const float og = 1.0f - __builtin_amdgcn_rcpf(1.0f + EXP2(-LOG2E * (tgate[0] + tgate[1])));
#pragma unroll
    for (int j = 0; j < 8; ++j) gld16_sc1(oa[j], of + j * 1024, loff);
#pragma unroll
    for (int t = 0; t < 2; ++t)
#pragma unroll
        for (int i = 0; i < 4; ++i) {
            const f32x4 v = xch[(wid ^ 1) * 512 + (t * 4 + i) * 64 + lane];
#pragma unroll
            for (int j = 0; j < 4; ++j) acc[t][4 * i + j] = (acc[t][4 * i + j] + v[j]) * og;
        }
#pragma unroll
    for (int j = 0; j < 8; ++j) {
        if (j == 0) wait_vm1<7>(oa[j]); else if (j == 1) wait_vm1<6>(oa[j]); else if (j == 2) wait_vm1<5>(oa[j]);
        else if (j == 3) wait_vm1<4>(oa[j]); else if (j == 4) wait_vm1<3>(oa[j]); else if (j == 5) wait_vm1<2>(oa[j]);
        else if (j == 6) wait_vm1<1>(oa[j]); else wait_vm1<0>(oa[j]);
        acc[0] = MFMA(wa[j][0], oa[j], acc[0]);
        acc[1] = MFMA(wa[j][1], oa[j], acc[1]);
        SBAR();
    }
    STAMP(43);
#pragma unroll
    for (int t = 0; t < 2; ++t)
#pragma unroll
        for (int i = 0; i < 4; ++i) {
            f32x4 v = {acc[t][4 * i], acc[t][4 * i + 1], acc[t][4 * i + 2], acc[t][4 * i + 3]};
            xch[(wid ^ 1) * 512 + (t * 4 + i) * 64 + lane] = v;
        }
    bar();
    f32x4 ra = {}, rb4 = {};
#pragma unroll
    for (int k = 0; k < 4; ++k) {
        ra += xch[((2 * k + mh) ^ 1) * 512 + (kq * 2) * 64 + lane];
        rb4 += xch[((2 * k + mh) ^ 1) * 512 + (kq * 2 + 1) * 64 + lane];
    }
    const u32x4 ov = {cvtpk(ra[0], ra[1]), cvtpk(ra[2], ra[3]), cvtpk(rb4[0], rb4[1]), cvtpk(rb4[2], rb4[3])};
    {
        const unsigned oo = (mb * 64 + mh * 32 + r) * 1024 + (nb * 64 + (kq >> 1) * 32 + h * 16 + (kq & 1) * 8) * 2;
        asm volatile("global_store_dwordx4 %0, %1, %2" :: "v"(oo), "v"(ov), "s"(out) : "memory");
    }
    if (CPF) asm volatile(".Lcode_end:");
}

extern "C" void kernel_launch(void* const* d_in, const int* in_sizes, int n_in, void* d_out, int out_size, void* d_ws,
                              size_t ws_size, hipStream_t stream) {
    (void)hipFuncSetAttribute((const void*)fused, hipFuncAttributeMaxDynamicSharedMemorySize, LDS_BYTES);
    fused<<<256, 512, LDS_BYTES, stream>>>((const u16*)d_in[0], (const u16*)d_in[1], (const u16*)d_in[2], (const u16*)d_in[4],
                                           (const u16*)d_in[5], (const u16*)d_in[6], (const u16*)d_in[7], (char*)d_ws + WSOFF, (u16*)d_out);
}
```

```cpp
#define MA 0
#define MB 59
#define MK 8
#include <hip/hip_runtime.h>
#include <hip/hip_bf16.h>
#include <stdint.h>
#include <type_traits>

typedef unsigned short u16;
typedef unsigned long long u64;
typedef __attribute__((ext_vector_type(8))) __bf16 bf16x8;
typedef __attribute__((ext_vector_type(2))) __bf16 bf16x2;
typedef __attribute__((ext_vector_type(4))) short s16x4;
typedef __attribute__((ext_vector_type(16))) float f32x16;
typedef __attribute__((ext_vector_type(4))) float f32x4;
typedef __attribute__((ext_vector_type(2))) float f32x2;
typedef __attribute__((ext_vector_type(4))) unsigned u32x4;
#define LDSP __attribute__((address_space(3)))
typedef LDSP char* ldsptr;
typedef __attribute__((address_space(1))) unsigned gu32;
typedef __attribute__((address_space(1))) u64 gu64;

constexpr int S_ = 2048, F_ = 1376, NH = 32;
constexpr int NTT = S_ / 32;
constexpr int NKF = F_ / 16;
constexpr float LOG2E = 1.4426950408889634f;
constexpr float QSCALE = 0.25f * LOG2E;

constexpr size_t WS_EP = 0;
constexpr size_t WS_FQ = 1024;
constexpr size_t WS_FH = WS_FQ + 2048;
constexpr size_t WS_FO = WS_FH + 2048;
constexpr size_t WS_G = WS_FO + 16384;
constexpr size_t WS_Q = 262144;
constexpr size_t WS_K = WS_Q + (size_t)NH * NTT * 1024;
constexpr size_t WS_V = WS_K + (size_t)NH * NTT * 1024;
constexpr size_t WS_O = WS_V + (size_t)NH * NTT * 1024;
constexpr size_t WS_H = WS_O + (size_t)NTT * 32 * 1024;

constexpr size_t WS_DUMP = WS_H + (size_t)NTT * NKF * 1024;

constexpr int LDS_BYTES = 163840;
constexpr int SLOT = 28672;
__host__ __device__ constexpr int phys(int st) { return 20480 + ((st + 4) % 5) * SLOT; }
constexpr int F1OFF = 4064, F1UP = 12320;
constexpr int CHK = 16512, KOFF = 8320;
constexpr int WO_LO = 130048, POLL = 162816, XCH = 49536, RING = 6144;

#define MFMA(a, b, c) __builtin_amdgcn_mfma_f32_32x32x16_bf16((a), (b), (c), 0, 0, 0)
#define OUTM 0
#define NODMA 0
#define NOGATE 0
#define ROT 1
#define KROT 1
#define IBAR 4
#define POLLG 18
#define SPIN(x) asm volatile("" : "+s"(x))
#define SBAR() __builtin_amdgcn_sched_barrier(0)
#define EXP2(x) __builtin_amdgcn_exp2f(x)
#define RLX_AGENT __ATOMIC_RELAXED, __HIP_MEMORY_SCOPE_AGENT
__device__ __forceinline__ u64 rt() { u64 t; asm volatile("s_memrealtime %0\n\ts_waitcnt lgkmcnt(0)" : "=s"(t) :: "memory"); return t; }
__device__ __forceinline__ u64 ct() { u64 t; asm volatile("s_memtime %0\n\ts_waitcnt lgkmcnt(0)" : "=s"(t) :: "memory"); return t; }
#define STAMP(i) do { if ((i) == MA) { SBAR(); mta = (unsigned)rt(); SBAR(); } if ((i) == MB) { SBAR(); mtb = (unsigned)rt(); SBAR(); } } while (0)
#define STAMPC(i)

__device__ __forceinline__ unsigned cvtpk(float lo, float hi) {
    f32x2 v = {lo, hi};
    return __builtin_bit_cast(unsigned, __builtin_convertvector(v, bf16x2));
}
__device__ __forceinline__ void glds16(const void* sbase, unsigned voff, unsigned lds_s, int lds_i) {
    asm volatile("s_add_u32 m0, %2, %3\n\ts_nop 0\n\tglobal_load_lds_dwordx4 %0, %1" :: "v"(voff), "s"(sbase), "s"(lds_s), "i"(lds_i) : "memory", "scc");
}
__device__ __forceinline__ void glds16_sc1(const void* sbase, unsigned voff, unsigned lds_s, int lds_i) {
    asm volatile("s_add_u32 m0, %2, %3\n\ts_nop 0\n\tglobal_load_lds_dwordx4 %0, %1 sc1" :: "v"(voff), "s"(sbase), "s"(lds_s), "i"(lds_i) : "memory", "scc");
}
__device__ __forceinline__ void glds4_sc1(const void* sbase, unsigned voff, unsigned lds_s, int lds_i) {
    asm volatile("s_add_u32 m0, %2, %3\n\ts_nop 0\n\tglobal_load_lds_dword %0, %1 sc1" :: "v"(voff), "s"(sbase), "s"(lds_s), "i"(lds_i) : "memory", "scc");
}
__device__ __forceinline__ void gld16_sc1(bf16x8& r, const void* sbase, unsigned voff) {
    asm volatile("global_load_dwordx4 %0, %1, %2 sc1" : "=v"(r) : "v"(voff), "s"(sbase) : "memory");
}
__device__ __forceinline__ void gld16_sc1_tied(bf16x8& r, const void* sbase, unsigned voff) {
    asm volatile("global_load_dwordx4 %0, %1, %2 sc1" : "+v"(r) : "v"(voff), "s"(sbase) : "memory");
}
__device__ __forceinline__ void gld4_sc1(float& r, const void* sbase, unsigned voff) {
    asm volatile("global_load_dword %0, %1, %2 sc1" : "=v"(r) : "v"(voff), "s"(sbase) : "memory");
}
__device__ __forceinline__ void gst16_sc1(void* sbase, unsigned voff, bf16x8 v) {
    asm volatile("global_store_dwordx4 %0, %1, %2 sc1\n\ts_nop 1" :: "v"(voff), "v"(v), "s"(sbase) : "memory");
}
__device__ __forceinline__ void gst4_sc1(void* sbase, unsigned voff, float v) {
    asm volatile("global_store_dword %0, %1, %2 sc1" :: "v"(voff), "v"(v), "s"(sbase) : "memory");
}
template <int N> __device__ __forceinline__ void wait_vm() { asm volatile("s_waitcnt vmcnt(%0)" ::"i"(N) : "memory"); }
template <int N> __device__ __forceinline__ void wait_vm1(bf16x8& a) { asm volatile("s_waitcnt vmcnt(%1)" : "+v"(a) : "i"(N) : "memory"); }
template <int N> __device__ __forceinline__ void wait_vm2(bf16x8& a, bf16x8& b) {
    asm volatile("s_waitcnt vmcnt(%2)" : "+v"(a), "+v"(b) : "i"(N) : "memory");
}
__device__ __forceinline__ void bar() { asm volatile("s_waitcnt lgkmcnt(0)\n\ts_barrier" ::: "memory"); }
__device__ __forceinline__ ldsptr ladd(ldsptr p, int off) {
    unsigned r;
    asm volatile("v_add_u32 %0, %1, %2" : "=v"(r) : "i"(off), "v"((unsigned)(uintptr_t)p));
    return (ldsptr)(uintptr_t)r;
}
__device__ __forceinline__ bf16x8 lds_b128(ldsptr p) { return *(LDSP bf16x8*)p; }
__device__ __forceinline__ bf16x8 tr_frag(ldsptr p0, ldsptr p1) {
    s16x4 a = __builtin_amdgcn_ds_read_tr16_b64_v4i16((LDSP s16x4*)p0), b = __builtin_amdgcn_ds_read_tr16_b64_v4i16((LDSP s16x4*)p1);
    typedef __attribute__((ext_vector_type(8))) short s16x8;
    s16x8 v = {a[0], a[1], a[2], a[3], b[0], b[1], b[2], b[3]};
    return __builtin_bit_cast(bf16x8, v);
}
__device__ __forceinline__ bf16x8 pack8(const f32x16& a, int o, float sc) {
    u32x4 v = {cvtpk(a[o] * sc, a[o + 1] * sc), cvtpk(a[o + 2] * sc, a[o + 3] * sc),
               cvtpk(a[o + 4] * sc, a[o + 5] * sc), cvtpk(a[o + 6] * sc, a[o + 7] * sc)};
    return __builtin_bit_cast(bf16x8, v);
}
__device__ __forceinline__ bf16x8 pack8(const f32x16& a, int o) {
    u32x4 v = {cvtpk(a[o], a[o + 1]), cvtpk(a[o + 2], a[o + 3]), cvtpk(a[o + 4], a[o + 5]), cvtpk(a[o + 6], a[o + 7])};
    return __builtin_bit_cast(bf16x8, v);
}
__device__ __forceinline__ void spin_flags(const char* p, bool need, u64 tag) {
#pragma unroll 1
    for (int spin = 0; spin < (1 << 20); ++spin) {
        const u64 v = __hip_atomic_load((gu64*)p, RLX_AGENT);
        if (__all(!need || v == tag)) break;
        __builtin_amdgcn_s_sleep(2);
    }
}

__host__ __device__ constexpr int grp(int s, int nq) { return s <= 7 ? nq : 3; }
__host__ __device__ constexpr int younger(int s, int nq) {
    int n = 0;
    for (int t = s + 1; t <= (s == 0 ? 4 : s + 3); ++t) n += grp(t, nq);
    return n;
}
template <int NQ> __device__ __forceinline__ void wait_stage(int s) {
    if (s == 1) wait_vm<younger(1, NQ)>(); else if (s == 2) wait_vm<younger(2, NQ)>();
    else if (s == 3) wait_vm<younger(3, NQ)>(); else if (s == 4) wait_vm<younger(4, NQ)>();
    else if (s == 5) wait_vm<younger(5, NQ)>(); else if (s == 6) wait_vm<younger(6, NQ)>();
    else if (s == 7) wait_vm<younger(7, NQ)>(); else wait_vm<younger(8, NQ)>();
}
__device__ __forceinline__ void flag_store(void* sbase, unsigned voff, u64 v, unsigned mask) {
    asm volatile("s_mov_b32 exec_lo, %3\n\ts_mov_b32 exec_hi, 0\n\tglobal_store_dwordx2 %0, %1, %2 sc1\n\ts_mov_b64 exec, -1" :: "v"(voff), "v"(v), "s"(sbase), "s"(mask) : "memory");
}
#define PIN(x) asm volatile("" : "+v"(x))

__device__ __forceinline__ f32x16 attn_fallback(const char* kf, const char* vf, bf16x8 qf, int lane) {
    f32x16 o = {};
    float m = -INFINITY;
    const bool isv = ((lane >> 4) & 1) == 0;
    const u32x4 ones = {0x3f803f80u, 0x3f803f80u, 0x3f803f80u, 0x3f803f80u};
    const unsigned voff = (lane >> 5) * 256 + (lane & 15) * 16;
#pragma unroll 1
    for (int t = 0; t < NTT; ++t) {
        bf16x8 kk, a0, a1;
        gld16_sc1(kk, kf + t * 1024, lane * 16);
        gld16_sc1(a0, vf + t * 1024, voff);
        gld16_sc1(a1, vf + t * 1024 + 512, voff);
        wait_vm1<0>(kk);
        wait_vm2<0>(a0, a1);
        u32x4 v0 = __builtin_bit_cast(u32x4, a0), v1 = __builtin_bit_cast(u32x4, a1);
        if (!isv) { v0 = ones; v1 = ones; }
        f32x16 z = {};
        f32x16 s = MFMA(kk, qf, z);
        float tm = s[0];
#pragma unroll
        for (int i = 1; i < 16; ++i) tm = fmaxf(tm, s[i]);
        tm = fmaxf(tm, __shfl_xor(tm, 32));
        const float mn = fmaxf(m, tm);
        const float al = EXP2(m - mn);
        m = mn;
#pragma unroll
        for (int i = 0; i < 16; ++i) { o[i] *= al; s[i] = EXP2(s[i] - mn); }
        o = MFMA(__builtin_bit_cast(bf16x8, v0), pack8(s, 0), o);
        o = MFMA(__builtin_bit_cast(bf16x8, v1), pack8(s, 8), o);
    }
    return o;
}

__global__ __launch_bounds__(512, 2) void fused(const u16* __restrict__ x, const u16* __restrict__ wqkv, const u16* __restrict__ wo,
                                                const u16* __restrict__ wg, const u16* __restrict__ wu, const u16* __restrict__ wd,
                                                const u16* __restrict__ gatew, char* __restrict__ ws, u16* __restrict__ out) {
    extern __shared__ __attribute__((aligned(16))) char smem[];
    unsigned mta = 0, mtb = 0;
    STAMP(0); STAMPC(61);
    asm volatile("" :: "s"(x), "s"(wqkv)); STAMP(59);
    const int tid = threadIdx.x, lane = tid & 63;
    const int wid = __builtin_amdgcn_readfirstlane(tid >> 6);
    const int xcd = blockIdx.x & 7, idx = blockIdx.x >> 3;
    const unsigned lds0 = 0;
    const unsigned loff = lane * 16;
    const int r = lane & 31, h = lane >> 5, q = (lane & 15) >> 2, p4 = lane & 3, blk = (lane >> 4) & 1;
    const int rb = (xcd & 1) * 8 + (idx & 7), cg = (xcd >> 1) * 4 + (idx >> 3);
    const int tt = wid & 3, ch = wid >> 2, hg = cg >> 1, odd = cg & 1;
    const int rot = ROT * ((idx & 7) + 2 * (idx >> 3));
    unsigned xoff[2], woff[2], foff[3];
#pragma unroll
    for (int e = 0; e < 2; ++e) {
        const int row = wid * 16 + e * 8 + (lane >> 3);
        xoff[e] = (unsigned)((rb * 128 + row) * 1024 + (((lane & 7) ^ ((row >> 1) & 7)) * 16));
    }
    const int wp0 = ch ? 2 * wid - 4 : wid;
#pragma unroll
    for (int e = 0; e < 2; ++e) {
        const int n = (wp0 + e) * 64 + lane, kr = n / 12, c = n % 12;
        woff[e] = (unsigned)(kr * 3072 + hg * 128 + (c < 8 ? odd * 2048 + c * 16 : 1024 - 128 + odd * 64 + c * 16));
    }
    unsigned ldx = lds0 + wid * 2048, ldw = lds0 + wp0 * 1024;
    SPIN(ldx); SPIN(ldw);
    auto issue_q = [&](int st, int op) {
        const int kb = (st + rot) & 7;
        if (op == 0) glds16((const char*)x + kb * 128, xoff[0], ldx, phys(st));
        if (op == 1) glds16((const char*)wqkv + kb * (64 * 3072), woff[0], ldw, phys(st) + 16384);
        if (op == 2) glds16((const char*)x + kb * 128, xoff[1], ldx, phys(st) + 1024);
        if (op == 3) glds16((const char*)wqkv + kb * (64 * 3072), woff[1], ldw, phys(st) + 16384 + 1024);
    };
    if (wid == 3) glds16(gatew, (loff + rot * 128) & 1023, lds0, 0);
#pragma unroll
    for (int st = 0; st < 5; ++st) {
        issue_q(st, 0); issue_q(st, 1); issue_q(st, 2);
        if (ch) issue_q(st, 3);
        if (st < IBAR) asm volatile("s_barrier" ::: "memory");
    }
    STAMP(1);
    const u64 ep = *(const __attribute__((address_space(4))) u64*)(uintptr_t)(ws + WS_EP);
    const u64 tag = (ep ^ (ep >> 31)) * 0x9E3779B97F4A7C15ull + 0xD1B54A32D192ED03ull;
    unsigned lead = __builtin_amdgcn_readfirstlane(tid < 64 ? 1 : 0);
    SPIN(lead);
    const int u0 = cg < 14 ? cg * 6 : 80;
#pragma unroll
    for (int e = 0; e < 3; ++e) {
        const int n = (tt * 3 + e) * 64 + lane, kr = n / 12, c = n % 12;
        foff[e] = (unsigned)(kr * (F_ * 2) + u0 * 32 + c * 16);
    }
    const char* fsrc = (const char*)(ch ? wu : wg);
    unsigned fdst = lds0 + F1OFF + ch * F1UP + tt * 3072;
    SPIN(fdst);
    auto issue_f = [&](int st, int op) {
        if (op < 3) glds16(fsrc + ((st + rot) & 7) * (64 * F_ * 2), foff[op], fdst, phys(st) + op * 1024);
    };
    bf16x8 xr[32];
    const int T = rb * 4 + tt;
    bf16x8 fw[3][3];
    ldsptr frd = (ldsptr)smem + F1OFF + (8 * h + q) * 192 + blk * F1UP + p4 * 8 + ch * 96, fs_;
    auto frd_ = [&](int g) {
        if ((g & 3) == 0) fs_ = ladd(frd, phys(8 + (g >> 2)));
        const int so = (g & 3) * 3072;
#pragma unroll
        for (int u = 0; u < 3; ++u) fw[g % 3][u] = tr_frag(fs_ + so + u * 32, fs_ + so + u * 32 + 768);
    };
    {
        ldsptr xrd[4];
#pragma unroll
        for (int ks = 0; ks < 4; ++ks) xrd[ks] = (ldsptr)smem + (tt * 32 + r) * 128 + (((2 * ks + h) ^ ((r >> 1) & 7)) * 16);
        ldsptr wrd = (ldsptr)smem + 16384 + (8 * h + q) * 192 + blk * 32 + p4 * 8 + ch * 128;
        ldsptr grd = (ldsptr)smem + h * 16;
        auto qkv = [&](auto var) {
            constexpr int VAR = decltype(var)::value;
            constexpr int NQ = VAR == 2 ? 4 : 3;
            f32x16 a0 = {}, a1 = {};
            float gacc = 0.f;
            bf16x8 w0[3], w1[3];
            ldsptr ws_;
            auto rd = [&](int g) {
                const int so = phys(g >> 2), ks = g & 3;
                if (ks == 0) ws_ = ladd(wrd, so);
                xr[g] = lds_b128(ladd(xrd[ks], so));
                w0[g % 3] = tr_frag(ws_ + ks * 3072, ws_ + ks * 3072 + 768);
                if (VAR != 2) w1[g % 3] = tr_frag(ws_ + ks * 3072 + 64, ws_ + ks * 3072 + 64 + 768);
                else w1[g % 3] = lds_b128(grd + g * 32);
            };
            wait_vm<younger(0, NQ)>();
            bar();
            STAMP(2);
            rd(0);
            rd(1);
#pragma unroll
            for (int g = 0; g < 32; ++g) {
                const int s = (g + 2) >> 2, op = (g + 2) & 3;
                if (op == 0) {
                    wait_stage<NQ>(s);
                    bar();
                    STAMP(2 + s);
                }
                if (g < 30) rd(g + 2); else frd_(g - 30);
                SBAR();
                if (VAR == 1) { a0 = MFMA(xr[g], w0[g % 3], a0); a1 = MFMA(xr[g], w1[g % 3], a1); }
                else { a0 = MFMA(w0[g % 3], xr[g], a0); if (VAR == 0) a1 = MFMA(w1[g % 3], xr[g], a1); }
                PIN(a0); if (VAR != 2) PIN(a1);
                SBAR();
                if (s >= 1 && !NODMA) { if (s + 4 <= 7) { if (op < NQ) issue_q(s + 4, op); } else issue_f(s + 4, op); }
                if (VAR == 2 && !NOGATE) {
#pragma unroll
                    for (int j = 0; j < 4; ++j) {
                        const bf16x2 xa = {xr[g][2 * j], xr[g][2 * j + 1]}, ga = {w1[g % 3][2 * j], w1[g % 3][2 * j + 1]};
                        gacc = __builtin_amdgcn_fdot2_f32_bf16(xa, ga, gacc, false);
                    }
                    PIN(gacc);
                }
                SBAR();
            }
            STAMP(11);
            if (VAR == 0) {
                char* qp = ws + WS_Q + ((size_t)(4 * hg) * NTT + T) * 1024;
                gst16_sc1(qp, loff, pack8(a0, 0, QSCALE));
                gst16_sc1(qp + NTT * 1024, loff, pack8(a0, 8, QSCALE));
                gst16_sc1(qp + 2 * NTT * 1024, loff, pack8(a1, 0, QSCALE));
                gst16_sc1(qp + 3 * NTT * 1024, loff, pack8(a1, 8, QSCALE));
            } else if (VAR == 1) {
                char* vp = ws + WS_V + ((size_t)(4 * hg) * NTT + T) * 1024;
                const unsigned vo = blk * (NTT * 1024) + h * 256 + (lane & 15) * 16;
                gst16_sc1(vp, vo, pack8(a0, 0));
                gst16_sc1(vp + 512, vo, pack8(a0, 8));
                gst16_sc1(vp + 2 * NTT * 1024, vo, pack8(a1, 0));
                gst16_sc1(vp + 2 * NTT * 1024 + 512, vo, pack8(a1, 8));
            } else {
                char* kp = ws + WS_K + ((size_t)(4 * hg + 2 * odd) * NTT + T) * 1024;
                gst16_sc1(kp, loff, pack8(a0, 0));
                gst16_sc1(kp + NTT * 1024, loff, pack8(a0, 8));
                gacc += __shfl_xor(gacc, 32);
                gst4_sc1(ws + WS_G + (cg * S_ + T * 32) * 4, r * 4, __builtin_amdgcn_rcpf(1.0f + EXP2(-LOG2E * gacc)));
            }
        };
        if (ch) qkv(std::integral_constant<int, 2>{});
        else if (odd) qkv(std::integral_constant<int, 1>{});
        else qkv(std::integral_constant<int, 0>{});
    }
    const int head = xcd * 4 + (idx & 3), qb = idx >> 2;
    const int AT = qb * 8 + wid;
    const int krot = KROT * 8 * qb;
    const char* kf = ws + WS_K + (size_t)head * NTT * 1024;
    const char* vf = ws + WS_V + (size_t)head * NTT * 1024;
    const bool isv = blk == 0;
    const unsigned voff = h * 256 + (lane & 15) * 16;
    const u32x4 ones = {0x3f803f80u, 0x3f803f80u, 0x3f803f80u, 0x3f803f80u};
    const int mb = xcd * 4 + (idx & 3), nb = idx >> 2;
    const int mh = wid & 1, kq = wid >> 1;
    bf16x8 qf, k0, kk, g0, g1;
    float agate, tgate;
    unsigned ldv = lds0 + wid * 1040, ldk = lds0 + wid * 1024;
    SPIN(ldv); SPIN(ldk);
    auto issue_kv = [&](int c, int op) {
        if (op == 0) glds16_sc1(vf + ((8 * c + 1 + wid + krot) & 63) * 1024, loff, ldv, c * CHK);
        else glds16_sc1(kf + ((8 * c + 2 + wid + krot) & 63) * 1024, loff, ldk, c * CHK + KOFF);
    };
    f32x16 o, acc[4];
    bf16x8 ha[3], hb[3];
    {
        f32x16 fa[3] = {};
        u64 pollv = 0;
        const char* fq = ws + WS_FQ + xcd * 256;
        STAMP(12);
#pragma unroll
        for (int g = 0; g < 32; ++g) {
            const int s = 8 + ((g + 2) >> 2), op = (g + 2) & 3;
            if (op == 0 && s <= 14) {
                if (s == 9) wait_vm<12>();
                else if (s == 10) wait_vm<4>();
                else if (s == 12) wait_vm<9>();
                else if (s == 13) wait_vm<6>();
                else if (s == 14) {
                    asm volatile("s_waitcnt vmcnt(0)" : "+v"(pollv) :: "memory");
                    if (wid == 0 && !__all(pollv == tag)) spin_flags(fq + r * 8, true, tag);
                }
                bar();
                STAMP(4 + s);
                if (s == 10) flag_store(ws + WS_FQ + hg * 256 + (odd * 16 + rb) * 8, 0, tag, lead);
            }
            if (g < 30) frd_(g + 2);
            SBAR();
#pragma unroll
            for (int u = 0; u < 3; ++u) fa[u] = MFMA(fw[g % 3][u], xr[g], fa[u]);
            PIN(fa[0]); PIN(fa[1]); PIN(fa[2]);
            SBAR();
            if (s <= 11 && !NODMA) issue_f(s + 4, op);
            if (g == POLLG) asm volatile("global_load_dwordx2 %0, %1, %2 sc1" : "+v"(pollv) : "v"(r * 8), "s"(fq) : "memory");
            if (g == 22) { gld16_sc1(qf, ws + WS_Q + ((size_t)head * NTT + AT) * 1024, loff); gld16_sc1(k0, kf + krot * 1024, loff); }
            if (g == 23) { gld16_sc1(g0, vf + krot * 1024, voff); gld16_sc1(g1, vf + krot * 1024 + 512, voff); }
            if (g == 24) { gld16_sc1(kk, kf + (krot + 1) * 1024, loff); issue_kv(0, 0); }
            if (g == 25) { issue_kv(0, 1); gld4_sc1(agate, ws + WS_G + (2 * xcd * S_ + AT * 32) * 4, r * 4); }
            if (g >= 26) issue_kv(1 + (g - 26) / 2, g & 1);
            if (g == 26) gld4_sc1(tgate, ws + WS_G + (2 * xcd * S_ + (2 * mb + mh) * 32) * 4, r * 4);
            if (g == 22) *(LDSP u32x4*)((ldsptr)smem + min(wid, 5) * CHK + (lane & 7) * 1040 + 1024) = ones;
            if (g >= 24 && g < 28) { acc[g - 24] = f32x16{}; PIN(acc[g - 24]); }
            if (g == 28) { o = f32x16{}; PIN(o); }
            if (g >= 29) { ha[g - 29] = bf16x8{}; hb[g - 29] = bf16x8{}; PIN(ha[g - 29]); PIN(hb[g - 29]); }
            SBAR();
        }
        STAMP(20);
        char* hp = ws + WS_H + ((size_t)T * NKF + u0 + ch * 3) * 1024;
#pragma unroll
        for (int u = 0; u < 3; ++u) {
            f32x16 t;
#pragma unroll
            for (int j = 0; j < 8; ++j) {
                const float g = fa[u][j];
                t[j] = g * fa[u][8 + j] * __builtin_amdgcn_rcpf(1.0f + EXP2(-LOG2E * g));
            }
            const bool own = cg < 14 || (cg == 14 && ch * 3 + u >= 4);
            gst16_sc1(own ? hp + u * 1024 : ws + WS_DUMP + (blockIdx.x * 8 + wid) * 1024, loff, pack8(t, 0));
        }
    }
    const int fk0 = wid * 11 - (wid > 6 ? 1 : 0) - (wid < 6 ? 1 : 2);
    unsigned doff;
    {
        const int row = lane >> 3;
        doff = (unsigned)(row * 1024 + nb * 128 + (((lane & 7) ^ (((row >> 1) & 1) << 2)) * 16));
    }
    const int row0 = 4 * h + q, sw = (row0 >> 1) & 1;
    const int wfo0 = row0 * 128 + sw * 64 + (p4 & 1) * 32 + blk * 16 + (p4 >> 1) * 8, wfo1 = wfo0 ^ 64;
    const char* hfa = ws + WS_H + ((ptrdiff_t)(2 * mb + mh) * NKF + fk0) * 1024;
    const char* hfb = ws + WS_H + ((ptrdiff_t)(2 * mb + 1 - mh) * NKF + fk0) * 1024;
    const char* wdp = (const char*)wd + (ptrdiff_t)fk0 * 16384;
    {
        asm volatile("s_waitcnt vmcnt(11)" : "+v"(qf), "+v"(k0), "+v"(kk), "+v"(g0), "+v"(g1) :: "memory");
        STAMP(21);
        f32x16 z = {};
        f32x16 s = MFMA(k0, qf, z);
        bar();
        float m = fmaxf(s[0], s[1]);
#pragma unroll
        for (int i = 2; i < 16; i += 2) m = fmaxf(fmaxf(m, s[i]), s[i + 1]);
        m = fmaxf(m, __shfl_xor(m, 32));
        f32x16 negm;
#pragma unroll
        for (int i = 0; i < 16; ++i) negm[i] = -m;
        s = MFMA(k0, qf, negm);
        bf16x8 v0 = isv ? g0 : __builtin_bit_cast(bf16x8, ones), v1 = isv ? g1 : __builtin_bit_cast(bf16x8, ones);
        u32x4 p1 = {0, 0, 0, 0};
        bf16x8 v1p = v1;
        ldsptr krd = (ldsptr)smem + KOFF + lane * 16;
        ldsptr vrd0 = (ldsptr)smem + (isv ? voff : 1024);
        ldsptr vrd1 = (ldsptr)smem + (isv ? 512 + voff : 1024);
        ldsptr wr0 = (ldsptr)smem + wid * RING + wfo0, wr1 = (ldsptr)smem + wid * RING + wfo1;
        unsigned ringb = lds0 + wid * RING;
        SPIN(ringb);
        auto refill = [&](int e, const char* wsrc, int n, u64 mask) {
            glds16(wsrc, doff, ringb, e * 2048);
            glds16(wsrc + 8192, doff, ringb, e * 2048 + 1024);
            asm volatile("s_mov_b64 exec, %5\n\tglobal_load_dwordx4 %0, %2, %3 sc1\n\tglobal_load_dwordx4 %1, %2, %4 sc1\n\ts_mov_b64 exec, -1"
                         : "+v"(ha[e]), "+v"(hb[e]) : "v"(loff), "s"(hfa + n * 1024), "s"(hfb + n * 1024), "s"(mask) : "memory");
        };
        auto chunk = [&](auto fused_, const char* ws0, int wst0, int wst, int n0, u64 m0_, u64 m1_, u64 m2_) {
            constexpr bool FU = decltype(fused_)::value;
            bf16x8 wf;
#pragma unroll
            for (int i = 0; i < 8; ++i) {
                const int e = i / 3, ph = i % 3;
                SBAR();
                f32x16 sn = MFMA(kk, qf, negm);
                SBAR();
                kk = lds_b128(krd + i * 1024);
                bf16x8 nv0 = lds_b128(vrd0 + i * 1040);
                if (FU && ph == 0) wait_vm2<8>(ha[e], hb[e]);
                if (FU && ph < 2) wf = tr_frag((ph ? wr1 : wr0) + e * 2048, (ph ? wr1 : wr0) + e * 2048 + 1024);
                SBAR();
                u32x4 p0;
                { float a = EXP2(s[0]), b2 = EXP2(s[1]), c2 = EXP2(s[2]), d = EXP2(s[3]); p0[0] = cvtpk(a, b2); p0[1] = cvtpk(c2, d); }
                SBAR();
                o = MFMA(v1p, __builtin_bit_cast(bf16x8, p1), o);
                SBAR();
                v1p = v1;
                v1 = lds_b128(vrd1 + i * 1040);
                SBAR();
                { float a = EXP2(s[4]), b2 = EXP2(s[5]), c2 = EXP2(s[6]), d = EXP2(s[7]); p0[2] = cvtpk(a, b2); p0[3] = cvtpk(c2, d); }
                SBAR();
                float e8 = EXP2(s[8]), e9 = EXP2(s[9]);
                SBAR();
                o = MFMA(v0, __builtin_bit_cast(bf16x8, p0), o);
                SBAR();
                v0 = nv0;
                { float c2 = EXP2(s[10]), d = EXP2(s[11]); p1[0] = cvtpk(e8, e9); p1[1] = cvtpk(c2, d); }
                SBAR();
                if (FU && ph < 2) acc[ph] = MFMA(wf, ha[e], acc[ph]);
                SBAR();
                float e12 = EXP2(s[12]), e13 = EXP2(s[13]), e14 = EXP2(s[14]), e15 = EXP2(s[15]);
                SBAR();
                if (FU && ph < 2) acc[2 + ph] = MFMA(wf, hb[e], acc[2 + ph]);
                SBAR();
                p1[2] = cvtpk(e12, e13); p1[3] = cvtpk(e14, e15);
                SBAR();
                if (FU && (ph == 2 || i == 7)) refill(e, ws0 + (e == 0 ? wst0 : e == 1 ? 16384 : wst), n0 + e, e == 0 ? m0_ : e == 1 ? m1_ : m2_);
                SBAR();
                s = sn;
            }
            krd += CHK; vrd0 += CHK; vrd1 += CHK;
        };
        STAMP(22);
#pragma unroll 1
        for (int c = 0; c < 3; ++c) {
            if (c == 0) {
                issue_kv(4, 0); issue_kv(4, 1); issue_kv(5, 0); issue_kv(5, 1);
                if (wid == 7 && lane >= 48) *(LDSP u32x4*)((ldsptr)smem + (lane >> 3) * CHK + (lane & 7) * 1040 + 1024) = ones;
            } else if (c == 1) {
                wait_vm<4>();
                bar();
                flag_store(ws + WS_FH + rb * 128 + cg * 8, 0, tag, lead);
                issue_kv(6, 0); issue_kv(6, 1);
                if (wid < 7) issue_kv(7, 0);
                if (wid < 6) issue_kv(7, 1);
            } else {
#pragma unroll
                for (int e = 0; e < 4; ++e) glds16((const char*)wo + (wid * 4 + e) * 8192, doff, ldk * 4, WO_LO + e * 1024);
                if (wid == 0) glds4_sc1(ws + WS_FH + (mb >> 1) * 128, r * 4, lds0, POLL);
            }
            STAMP(30 + c);
            chunk(std::false_type{}, nullptr, 0, 0, 0, 0, 0, 0);
        }
        wait_vm<0>();
        if (wid == 0) {
            const u64 v = *(LDSP u64*)((ldsptr)smem + POLL + (lane & 15) * 8);
            if (!__all((lane & 15) == 15 || v == tag)) spin_flags(ws + WS_FH + (mb >> 1) * 128 + (lane & 15) * 8, (lane & 15) != 15, tag);
        }
        bar();
#pragma unroll 1
        for (int c = 3; c < 8; ++c) {
            STAMP(30 + c);
            const int n0 = c < 7 ? 3 * c - 9 : 2;
            const char* w0 = c < 7 ? wdp + n0 * 16384 : (const char*)wo + (16 + 2 * wid) * 16384;
            const u64 ml = c < 7 ? ~0ull : 1ull;
            chunk(std::true_type{}, w0, c == 3 ? 16384 : 0, c < 7 ? 32768 : 0, n0, c == 3 ? 0ull : ml, c == 3 && wid >= 6 ? 0ull : ml, ml);
        }
        o = MFMA(v1p, __builtin_bit_cast(bf16x8, p1), o);
        asm volatile("s_waitcnt vmcnt(0)" : "+v"(ha[0]), "+v"(hb[0]), "+v"(ha[1]), "+v"(hb[1]), "+v"(ha[2]), "+v"(hb[2]), "+v"(agate), "+v"(tgate) :: "memory");
    }
    STAMP(40);
    {
        float chk = 0.f;
#pragma unroll
        for (int i = 0; i < 8; ++i) chk += fabsf(o[i]) * 0.125f;
        const bool bad = !(o[8] <= 1.8446744e19f) || !(chk <= 3.0e38f);
        if (__builtin_expect(__any(bad), 0)) o = attn_fallback(kf, vf, qf, lane);
        const float rl = agate * __builtin_amdgcn_rcpf(o[8]);
        gst16_sc1(ws + WS_O + ((size_t)AT * NH + head) * 1024, loff, pack8(o, 0, rl));
        wait_vm<0>();
        bar();
        if (tid < 8) __hip_atomic_store((gu64*)(ws + WS_FO + tid * 2048 + qb * 256) + head, tag, RLX_AGENT);
    }
    STAMP(41);
    LDSP f32x4* xch = (LDSP f32x4*)((ldsptr)smem + XCH);
#pragma unroll
    for (int t = 0; t < 2; ++t)
#pragma unroll
        for (int i = 0; i < 4; ++i) {
            f32x4 v = {acc[2 + t][4 * i], acc[2 + t][4 * i + 1], acc[2 + t][4 * i + 2], acc[2 + t][4 * i + 3]};
            xch[wid * 512 + (t * 4 + i) * 64 + lane] = v;
        }
    bf16x8 wa[8][2];
    {
        const int base = kq < 2 ? WO_LO + kq * 16384 : (kq - 2) * 4 * RING, st2 = kq < 2 ? 4096 : RING;
#pragma unroll
        for (int j = 0; j < 8; ++j) {
            ldsptr p = (ldsptr)smem + base + (j >> 1) * st2 + (j & 1) * 2048;
            wa[j][0] = tr_frag(p + wfo0, p + wfo0 + 1024);
            wa[j][1] = tr_frag(p + wfo1, p + wfo1 + 1024);
        }
    }
    if (wid == 0) spin_flags(ws + WS_FO + nb * 2048 + (mb >> 2) * 256 + r * 8, true, tag);
    if (wid == 1 && blockIdx.x == 0) {
#pragma unroll
        for (int i = 0; i < 4; ++i) spin_flags(ws + WS_FQ + i * 512 + lane * 8, true, tag);
        if (lane == 0) __hip_atomic_store((gu64*)(ws + WS_EP), tag, RLX_AGENT);
    }
    bar();
    STAMP(42);
    const char* of = ws + WS_O + ((size_t)(2 * mb + mh) * NH + kq * 8) * 1024;
    bf16x8 oa[8];
    const float og = 1.0f - tgate;
#pragma unroll
    for (int j = 0; j < 8; ++j) gld16_sc1(oa[j], of + j * 1024, loff);
#pragma unroll
    for (int t = 0; t < 2; ++t)
#pragma unroll
        for (int i = 0; i < 4; ++i) {
            const f32x4 v = xch[(wid ^ 1) * 512 + (t * 4 + i) * 64 + lane];
#pragma unroll
            for (int j = 0; j < 4; ++j) acc[t][4 * i + j] = (acc[t][4 * i + j] + v[j]) * og;
        }
#pragma unroll
    for (int j = 0; j < 8; ++j) {
        if (j == 0) wait_vm1<7>(oa[j]); else if (j == 1) wait_vm1<6>(oa[j]); else if (j == 2) wait_vm1<5>(oa[j]);
        else if (j == 3) wait_vm1<4>(oa[j]); else if (j == 4) wait_vm1<3>(oa[j]); else if (j == 5) wait_vm1<2>(oa[j]);
        else if (j == 6) wait_vm1<1>(oa[j]); else wait_vm1<0>(oa[j]);
        acc[0] = MFMA(wa[j][0], oa[j], acc[0]);
        acc[1] = MFMA(wa[j][1], oa[j], acc[1]);
        SBAR();
    }
    STAMP(43);
#pragma unroll
    for (int t = 0; t < 2; ++t)
#pragma unroll
        for (int i = 0; i < 4; ++i) {
            f32x4 v = {acc[t][4 * i], acc[t][4 * i + 1], acc[t][4 * i + 2], acc[t][4 * i + 3]};
            xch[(wid ^ 1) * 512 + (t * 4 + i) * 64 + lane] = v;
        }
    bar();
    f32x4 ra = {}, rb4 = {};
#pragma unroll
    for (int k = 0; k < 4; ++k) {
        ra += xch[((2 * k + mh) ^ 1) * 512 + (kq * 2) * 64 + lane];
        rb4 += xch[((2 * k + mh) ^ 1) * 512 + (kq * 2 + 1) * 64 + lane];
    }
    const u32x4 ov = {cvtpk(ra[0], ra[1]), cvtpk(ra[2], ra[3]), cvtpk(rb4[0], rb4[1]), cvtpk(rb4[2], rb4[3])};
    *(u32x4*)((char*)out + (size_t)(mb * 64 + mh * 32 + r) * 1024 + (nb * 64 + (kq >> 1) * 32 + h * 16 + (kq & 1) * 8) * 2) = ov;
    STAMP(50);
    if (MA < 60) { const unsigned te = (unsigned)rt() + MK * (mtb - mta); while ((int)((unsigned)rt() - te) < 0) __builtin_amdgcn_s_sleep(1); }
    else { const unsigned te = (unsigned)ct() + 40000; while ((int)((unsigned)ct() - te) < 0) __builtin_amdgcn_s_sleep(1); }
}

extern "C" void kernel_launch(void* const* d_in, const int* in_sizes, int n_in, void* d_out, int out_size, void* d_ws,
                              size_t ws_size, hipStream_t stream) {
    (void)hipFuncSetAttribute((const void*)fused, hipFuncAttributeMaxDynamicSharedMemorySize, LDS_BYTES);
    fused<<<256, 512, LDS_BYTES, stream>>>((const u16*)d_in[0], (const u16*)d_in[1], (const u16*)d_in[2], (const u16*)d_in[4],
                                           (const u16*)d_in[5], (const u16*)d_in[6], (const u16*)d_in[7], (char*)d_ws, (u16*)d_out);
}
```

```cpp
#include <hip/hip_runtime.h>
#include <hip/hip_bf16.h>
#include <stdint.h>
#include <type_traits>

typedef unsigned short u16;
typedef unsigned long long u64;
typedef __attribute__((ext_vector_type(8))) __bf16 bf16x8;
typedef __attribute__((ext_vector_type(2))) __bf16 bf16x2;
typedef __attribute__((ext_vector_type(4))) short s16x4;
typedef __attribute__((ext_vector_type(16))) float f32x16;
typedef __attribute__((ext_vector_type(4))) float f32x4;
typedef __attribute__((ext_vector_type(2))) float f32x2;
typedef __attribute__((ext_vector_type(4))) unsigned u32x4;
#define LDSP __attribute__((address_space(3)))
typedef LDSP char* ldsptr;
typedef __attribute__((address_space(1))) unsigned gu32;
typedef __attribute__((address_space(1))) u64 gu64;

constexpr int S_ = 2048, F_ = 1376, NH = 32;
constexpr int NTT = S_ / 32;
constexpr int NKF = F_ / 16;
constexpr float LOG2E = 1.4426950408889634f;
constexpr float QSCALE = 0.25f * LOG2E;

constexpr size_t WS_EP = 0;
constexpr size_t WS_G = 1024;
constexpr size_t WS_FH = 16384;
constexpr size_t WS_HT = 32768;
constexpr size_t WS_KV = 262144;
constexpr size_t WS_O = WS_KV + (size_t)64 * 65536;
constexpr size_t WS_H = WS_O + (size_t)NTT * 32 * 1024;
constexpr size_t WS_DUMP = WS_H + (size_t)NTT * NKF * 1024;
constexpr size_t WS_FK = WS_DUMP + (1u << 20);

constexpr int LDS_BYTES = 163840;
constexpr int SLOT = 28672;
__host__ __device__ constexpr int phys(int st) { return 20480 + (st + 2) % 5 * SLOT; }
constexpr int ROW = 16672;
__host__ __device__ constexpr int hphys(int hi) {
    return hi == 0 ? 0 : 20480 + (2 + (hi - 1) % 6 / 2) * SLOT + 4096 + ((hi - 1) & 1) * 12288;
}
constexpr int WB = 4 * ROW, REG = 22528;
constexpr int QX = WB, GATE = QX + 4096, MX = GATE + 4096, DUMP = 12288;
constexpr int CW = 16384 + 32;
constexpr int W_PA = CW, W_PF = CW + 16, W_CH = CW + 32, W_GH = CW + 36, W_MK = CW + 40, W_CT = CW + 64;
constexpr int C0 = 0x40000 + 12 * 4096;

#define MFMA(a, b, c) __builtin_amdgcn_mfma_f32_32x32x16_bf16((a), (b), (c), 0, 0, 0)
#define NI 2
#define RATE 12
#define FRATE 1
#define PRA 2
#define PRF 3
#define PRM 0
#define PF1 0
#define PFU PF1
#define PSI 3
#define FS 8
#define HP 3
#define PG 6
#define PS 13
#define PRT 3
#define PP 3
#define PC 3
#define PE 3
#define P2 0
#define L2P 0
#define OM " sc1"
#define WS 5
#define NOST 4
#define PB2 0
#define P2A "global_load_dword v194, %[po], %[hb] sc1"
#define X1 1
#define XS 1
#define FW 14
#define EO 0
#define PRX 1
#define EP 22
#define NF1 3
#define CAPQ 65
#define F0 0
#define GA 4
#define K6 3
#define RK 4
#define HD 6
#define SLQ 1
#define SLO 1
#define SPIN(x) asm volatile("" : "+s"(x))
#define SBAR() __builtin_amdgcn_sched_barrier(0)
#define EXP2(x) __builtin_amdgcn_exp2f(x)
#define RLX_AGENT __ATOMIC_RELAXED, __HIP_MEMORY_SCOPE_AGENT
#define STAMP(i)
#define STAMPC(i)

__device__ __forceinline__ unsigned cvtpk(float lo, float hi) {
    f32x2 v = {lo, hi};
    return __builtin_bit_cast(unsigned, __builtin_convertvector(v, bf16x2));
}
__device__ __forceinline__ void glds16(const void* sbase, unsigned voff, unsigned lds_s, int lds_i) {
    asm volatile("s_add_u32 m0, %2, %3\n\ts_nop 0\n\tglobal_load_lds_dwordx4 %0, %1" :: "v"(voff), "s"(sbase), "s"(lds_s), "i"(lds_i) : "memory", "scc");
}
__device__ __forceinline__ void glds16k(unsigned blo, unsigned bhi, int k, int stride, unsigned voff, unsigned lds_s, int lds_i) {
    asm volatile("s_add_u32 m0, %1, %2\n\ts_mul_i32 vcc_lo, %3, %4\n\ts_add_u32 vcc_lo, vcc_lo, %5\n\ts_addc_u32 vcc_hi, %6, 0\n\tglobal_load_lds_dwordx4 %0, vcc"
                 :: "v"(voff), "s"(lds_s), "i"(lds_i), "s"(k), "i"(stride), "s"(blo), "s"(bhi) : "memory", "scc", "vcc");
}
__device__ __forceinline__ void glds16_sc1(const void* sbase, unsigned voff, unsigned lds_s, int lds_i) {
    asm volatile("s_add_u32 m0, %2, %3\n\ts_nop 0\n\tglobal_load_lds_dwordx4 %0, %1 sc1" :: "v"(voff), "s"(sbase), "s"(lds_s), "i"(lds_i) : "memory", "scc");
}
__device__ __forceinline__ void glds4(const void* sbase, unsigned voff, unsigned lds_s, int lds_i) {
    asm volatile("s_add_u32 m0, %2, %3\n\ts_nop 0\n\tglobal_load_lds_dword %0, %1" :: "v"(voff), "s"(sbase), "s"(lds_s), "i"(lds_i) : "memory", "scc");
}
__device__ __forceinline__ void glds4_sc1(const void* sbase, unsigned voff, unsigned lds_s, int lds_i) {
    asm volatile("s_add_u32 m0, %2, %3\n\ts_nop 0\n\tglobal_load_lds_dword %0, %1 sc1" :: "v"(voff), "s"(sbase), "s"(lds_s), "i"(lds_i) : "memory", "scc");
}
__device__ __forceinline__ void blds16_sc1(u32x4 srd, unsigned voff, unsigned soff, int off) {
    asm volatile("buffer_load_dwordx4 %0, %1, %2 offen offset:%3 sc1 lds" :: "v"(voff), "s"(srd), "s"(soff), "i"(off) : "memory");
}
__device__ __forceinline__ void gld16_sc1(bf16x8& r, const void* sbase, unsigned voff) {
    asm volatile("global_load_dwordx4 %0, %1, %2 sc1" : "=v"(r) : "v"(voff), "s"(sbase) : "memory");
}
__device__ __forceinline__ void gld16_sc1_tied(bf16x8& r, const void* sbase, unsigned voff) {
    asm volatile("global_load_dwordx4 %0, %1, %2 sc1" : "+v"(r) : "v"(voff), "s"(sbase) : "memory");
}
__device__ __forceinline__ void gld8_sc1(f32x2& r, const void* sbase, unsigned voff) {
    asm volatile("global_load_dwordx2 %0, %1, %2 sc1" : "=v"(r) : "v"(voff), "s"(sbase) : "memory");
}
__device__ __forceinline__ void gst16_sc1(void* sbase, unsigned voff, bf16x8 v) {
    asm volatile("global_store_dwordx4 %0, %1, %2 sc1\n\ts_nop 1" :: "v"(voff), "v"(v), "s"(sbase) : "memory");
}
__device__ __forceinline__ void gst4_sc1(void* sbase, unsigned voff, float v) {
    asm volatile("global_store_dword %0, %1, %2 sc1" :: "v"(voff), "v"(v), "s"(sbase) : "memory");
}
template <int N> __device__ __forceinline__ void wait_vm() { asm volatile("s_waitcnt vmcnt(%0)" ::"i"(N) : "memory"); }
template <int N> __device__ __forceinline__ void wait_vm1(bf16x8& a) { asm volatile("s_waitcnt vmcnt(%1)" : "+v"(a) : "i"(N) : "memory"); }
template <int N> __device__ __forceinline__ void wait_vm2(bf16x8& a, bf16x8& b) {
    asm volatile("s_waitcnt vmcnt(%2)" : "+v"(a), "+v"(b) : "i"(N) : "memory");
}
__device__ __forceinline__ void bar() { asm volatile("s_waitcnt lgkmcnt(0)\n\ts_barrier" ::: "memory"); }
__device__ __forceinline__ ldsptr ladd(ldsptr p, int off) {
    unsigned r;
    asm volatile("v_add_u32 %0, %1, %2" : "=v"(r) : "i"(off), "v"((unsigned)(uintptr_t)p));
    return (ldsptr)(uintptr_t)r;
}
__device__ __forceinline__ bf16x8 lds_b128(ldsptr p) { return *(LDSP bf16x8*)p; }
__device__ __forceinline__ bf16x8 tr_frag(ldsptr p0, ldsptr p1) {
    s16x4 a = __builtin_amdgcn_ds_read_tr16_b64_v4i16((LDSP s16x4*)p0), b = __builtin_amdgcn_ds_read_tr16_b64_v4i16((LDSP s16x4*)p1);
    typedef __attribute__((ext_vector_type(8))) short s16x8;
    s16x8 v = {a[0], a[1], a[2], a[3], b[0], b[1], b[2], b[3]};
    return __builtin_bit_cast(bf16x8, v);
}
__device__ __forceinline__ bf16x8 pack8(const f32x16& a, int o, float sc) {
    f32x2 t0 = f32x2{a[o], a[o + 1]} * sc, t1 = f32x2{a[o + 2], a[o + 3]} * sc, t2 = f32x2{a[o + 4], a[o + 5]} * sc, t3 = f32x2{a[o + 6], a[o + 7]} * sc;
    asm volatile("" : "+v"(t0), "+v"(t1), "+v"(t2), "+v"(t3));
    u32x4 v = {cvtpk(t0[0], t0[1]), cvtpk(t1[0], t1[1]), cvtpk(t2[0], t2[1]), cvtpk(t3[0], t3[1])};
    return __builtin_bit_cast(bf16x8, v);
}
__device__ __forceinline__ bf16x8 pack8(const f32x16& a, int o) {
    u32x4 v = {cvtpk(a[o], a[o + 1]), cvtpk(a[o + 2], a[o + 3]), cvtpk(a[o + 4], a[o + 5]), cvtpk(a[o + 6], a[o + 7])};
    return __builtin_bit_cast(bf16x8, v);
}
template <int SL> __device__ __forceinline__ void spin_flags(const char* p, bool need, u64 tag) {
#pragma unroll 1
    for (int spin = 0; spin < (1 << 20); ++spin) {
        const u64 v = __hip_atomic_load((gu64*)p, RLX_AGENT);
        if (__all(!need || v == tag)) break;
        __builtin_amdgcn_s_sleep(SL);
    }
}

#define CLOB "v128", "v129", "v130", "v131", "v132", "v133", "v134", "v135", "v136", "v137", "v138", "v139", "v140", "v141", "v142", "v143", "v144", "v145", "v146", "v147", "v148", "v149", "v150", "v151", "v152", "v153", "v154", "v155", "v156", "v157", "v158", "v159", "v160", "v161", "v162", "v163", "v164", "v165", "v166", "v167", "v168", "v169", "v170", "v171", "v172", "v173", "v174", "v175", "v176", "v177", "v178", "v179", "v180", "v181", "v182", "v183", "v184", "v185", "v186", "v187", "v188", "v189", "v190", "v191", "v192", "v193"
#define PIN(x) asm volatile("" : "+v"(x))
#define CFENCE() asm volatile("" ::: "memory")
template <int I, int N, class F> __device__ __forceinline__ void sfor(F&& f) {
    if constexpr (I < N) { f(std::integral_constant<int, I>{}); sfor<I + 1, N>(f); }
}
__host__ __device__ constexpr int endi(int it) { return it < 8 ? 7 * (it + 1) : 56 + 6 * (it - 7); }
__host__ __device__ constexpr int endh(int hi) { return 59 + 3 * hi; }
__host__ __device__ constexpr int cum(int s) {
    int n = endi(NI - 1);
    for (int i = 0; i < s; ++i) { const int cap = endi(i == 4 ? 7 : i + 4); n = n + RATE < cap ? n + RATE : cap; }
    return s > 6 ? CAPQ : s == 6 ? 56 + K6 : n;
}
__host__ __device__ constexpr int cumf(int g) {
    int n = F0 > CAPQ ? F0 : CAPQ;
    for (int i = 0; i < g; ++i) {
        const int h = (i + 2) / 4 + 4, cap = endh(i < GA ? 2 : h < 6 ? 6 : h < 15 ? h : 15);
        n = n + FRATE < cap ? n + FRATE : cap;
    }
    return n;
}
__host__ __device__ constexpr int f2wait(int n) {
    int c = n < HD ? 2 * (HD - 1 - n) : 0;
    for (int m = n < HD ? 0 : n - HD + 1; m < n; ++m) c += (m <= 18 ? 2 : 0) + (m + HD < 22 ? 2 : 0);
    return c;
}
__device__ __forceinline__ void set_word(ldsptr p, unsigned val) {
    CFENCE();
    *(volatile LDSP unsigned*)p = val;
    CFENCE();
}
__device__ __forceinline__ unsigned get_word(ldsptr p) { return *(volatile LDSP unsigned*)p; }
template <int SL> __device__ __forceinline__ void spin_words(ldsptr p, unsigned need) {
#pragma unroll 1
    for (int spin = 0; spin < (1 << 22); ++spin) {
        if (!__any(get_word(p) < need)) break;
        if (SL) __builtin_amdgcn_s_sleep(SL);
    }
    CFENCE();
}
__device__ __forceinline__ void check_words(unsigned v, ldsptr p, unsigned need) {
    if (__builtin_expect(__any(v < need), 0)) spin_words<0>(p, need);
    CFENCE();
}
__device__ __forceinline__ bool arrive_last(int addr) {
    unsigned a, v;
    asm volatile("s_mov_b64 exec, 1\n\tv_mov_b32 %0, %2\n\tv_mov_b32 %1, 1\n\tds_add_rtn_u32 %1, %0, %1\n\ts_waitcnt lgkmcnt(0)\n\ts_mov_b64 exec, -1"
                 : "=&v"(a), "=&v"(v) : "i"(addr) : "memory");
    return __builtin_amdgcn_readfirstlane(v) == 3;
}

__device__ __forceinline__ f32x16 attn_fallback(const char* kv, bf16x8 qf, int lane) {
    f32x16 o = {};
    float m = -INFINITY;
    const bool isv = ((lane >> 4) & 1) == 0;
    const u32x4 ones = {0x3f803f80u, 0x3f803f80u, 0x3f803f80u, 0x3f803f80u};
    const unsigned voff = 2048 + (lane >> 5) * 512 + (lane & 15) * 16;
#pragma unroll 1
    for (int t = 0; t < NTT; ++t) {
        bf16x8 kk, a0, a1;
        gld16_sc1(kk, kv + t * 4096, lane * 16);
        gld16_sc1(a0, kv + t * 4096, voff);
        gld16_sc1(a1, kv + t * 4096 + 256, voff);
        wait_vm1<0>(kk);
        wait_vm2<0>(a0, a1);
        u32x4 v0 = __builtin_bit_cast(u32x4, a0), v1 = __builtin_bit_cast(u32x4, a1);
        if (!isv) { v0 = ones; v1 = ones; }
        f32x16 z = {};
        f32x16 s = MFMA(kk, qf, z);
        float tm = s[0];
#pragma unroll
        for (int i = 1; i < 16; ++i) tm = fmaxf(tm, s[i]);
        tm = fmaxf(tm, __shfl_xor(tm, 32));
        const float mn = fmaxf(m, tm);
        const float al = EXP2(m - mn);
        m = mn;
#pragma unroll
        for (int i = 0; i < 16; ++i) { o[i] *= al; s[i] = EXP2(s[i] - mn); }
        o = MFMA(__builtin_bit_cast(bf16x8, v0), pack8(s, 0), o);
        o = MFMA(__builtin_bit_cast(bf16x8, v1), pack8(s, 8), o);
    }
    return o;
}
template <int SL> __device__ __forceinline__ void spin_count(ldsptr p, unsigned need) {
#pragma unroll 1
    for (int spin = 0; spin < (1 << 22); ++spin) {
        if (!__any(get_word(p) > need)) break;
        if (SL) __builtin_amdgcn_s_sleep(SL);
    }
    CFENCE();
}

__global__ __launch_bounds__(512, 2) void fused(const u16* __restrict__ x_, const u16* __restrict__ wqkv_, const u16* __restrict__ wo_,
                                                const u16* __restrict__ wg_, const u16* __restrict__ wu_, const u16* __restrict__ wd_,
                                                const u16* __restrict__ gatew_, char* __restrict__ ws_a, u16* __restrict__ out_) {
    extern __shared__ __attribute__((aligned(16))) char smem[];
    STAMP(0); STAMPC(61);
    u64 pc;
    asm volatile("s_getpc_b64 %0\n.Lcode_begin:" : "=s"(pc));
    const int tid = threadIdx.x;
    const int wid = __builtin_amdgcn_readfirstlane(tid >> 6);
    const ldsptr lds = (ldsptr)smem;
#define GEOMETRY \
    SBAR(); \
    const int lane = tid & 63, xcd = blockIdx.x & 7, idx = blockIdx.x >> 3, tt = wid & 3; \
    const unsigned loff = lane * 16; \
    const int r = lane & 31, h = lane >> 5, q = (lane & 15) >> 2, p4 = lane & 3, blk = (lane >> 4) & 1; \
    const int rb = (xcd & 1) * 8 + (idx & 7), cg = (xcd >> 1) * 4 + (idx >> 3); \
    const int T = rb * 4 + tt; \
    ldsptr xrd[4]; \
    for (int ks = 0; ks < 4; ++ks) xrd[ks] = lds + (tt * 32 + r) * 128 + (((2 * ks + h) ^ ((r >> 1) & 7)) * 16); \
    const int wro = (8 * h + q) * 192 + blk * 32 + p4 * 8; \
    u64 ep;
    if (wid >= 4) {
        char* ws;
        asm volatile("s_load_dwordx2 %0, %1, 0x38" : "=&s"(ws) : "s"(__builtin_amdgcn_kernarg_segment_ptr()));
        GEOMETRY
        __builtin_amdgcn_s_setprio(PRA);
        if (wid == 4 && lane < 48) *(LDSP unsigned*)(lds + CW + lane * 4) = lane < 16 ? 0 : C0 + 4096;
        asm volatile("s_waitcnt lgkmcnt(0)" : "+s"(ws));
        asm volatile("s_load_dwordx2 %0, %1, 0\n\ts_waitcnt lgkmcnt(0)" : "=&s"(ep) : "s"(ws + WS_EP));
        u64 tag = (ep ^ (ep >> 31)) * 0x9E3779B97F4A7C15ull + 0xD1B54A32D192ED03ull, tagv = tag;
        const int hs = tt & 1;
        const bool isv = blk == 0, one = (lane & 11) == 0;
        const char* kvw = ws + WS_KV + (size_t)(cg * 4 + tt) * 65536;
        u32x4 srd = {(unsigned)(uintptr_t)kvw, (unsigned)((uintptr_t)kvw >> 32) & 0xffffu, 65536u, 0x00020000u};
        unsigned sx = 0x40000 | (rb ^ 15) << 12, rowb = tt * ROW;
        unsigned ko = loff + hs * 1024, kp = ko ^ 1024, vo = 2048 + (blk ^ hs) * 1024 + h * 512 + (lane & 15) * 16;
        unsigned kbase = 0x2000 + hs * 1024 + loff, kmask = 0x3000, vmask = isv ? 0x3000 : 0;
        unsigned vbase = isv ? 0x2000 + 2048 + hs * 1024 + h * 512 + (lane & 15) * 16 : one ? 16384 : 16400;
        unsigned cta = W_CT + r * 4, ctv = 512;
        char* kvo = (char*)kvw + rb * 4096;
        const char *fk = ws + WS_FK + (cg * 4 + tt) * (16 * FS), *fl = fk + (rb * RK >> 4) * (1024 * FS);
        unsigned fko = (lane & (RK - 1)) * (1024 * FS) + rb * FS, flo = (lane & 15) * FS;
        ldsptr lk0 = lds + tt * ROW + ko, lk1 = lds + tt * ROW + kp, lv = lds + tt * ROW + vo, lc = lds + tt * ROW + (lane & 8) * 32 + (lane & 7) * 4;
        ldsptr qw = lds + QX + tt * 1024 + loff, qr = lds + QX + (tt ^ 1) * 1024 + loff, mw = lds + MX + tt * 256 + lane * 4, mr = lds + MX + (tt ^ 1) * 256 + 128 + r * 4;
        ldsptr krd = (ldsptr)(uintptr_t)(kbase ^ 0x1000), vrd = (ldsptr)(uintptr_t)(vbase ^ (vmask & 0x1000)), w8 = lds + W_PA + (lane & 7) * 4, myw = lds + W_PA + tt * 4;
        char *opa = ws + WS_O + (size_t)(T * NH + 2 * cg + hs) * 1024, *opb = ws + WS_O + (size_t)((T ^ 1) * NH + 2 * cg + hs) * 1024;
        unsigned hto = WS_HT + (T >> 1) * 256 + (cg >> 3) * 128 + (cg & 7) * 4;
        u64 hm = hs ^ 1;
        unsigned cls = 0x207;
        SPIN(opa); SPIN(opb); SPIN(hm); PIN(hto); PIN(cls);
        unsigned cv = lane & 4 ? 0u : 0x3f803f80u, n2 = lane & 4 ? 0 : 2, two = 2;
        SPIN(srd); SPIN(sx); SPIN(rowb); SPIN(tag); SPIN(kvo); SPIN(fk); SPIN(fl);
        PIN(ko); PIN(kp); PIN(vo); PIN(kbase); PIN(kmask); PIN(vmask); PIN(vbase); PIN(cta); PIN(ctv); PIN(tagv); PIN(fko); PIN(flo);
        PIN(lk0); PIN(lk1); PIN(lv); PIN(lc); PIN(qw); PIN(qr); PIN(mw); PIN(mr); PIN(krd); PIN(vrd); PIN(w8); PIN(myw); PIN(cv); PIN(n2); PIN(two);
        bf16x8 qa, qc, kk[2], v0[2], v1[2];
        f32x16 sa, sc, na, nc, oa, ob;
        const f32x16 z = {};
        auto splat = [](f32x16& d, float v) {
            typedef __attribute__((ext_vector_type(8))) double f64x8;
            const f32x2 p = {v, v};
            f64x8 t;
#pragma unroll
            for (int i = 0; i < 8; ++i) { double e; asm volatile("v_mov_b64 %0, %1" : "=v"(e) : "v"(p)); t[i] = e; }
            d = __builtin_bit_cast(f32x16, t);
        };
        auto rowmax = [](const f32x16& s) {
            float m, n;
            asm("v_max3_f32 %0, %1, %2, %3" : "=v"(m) : "v"(s[0]), "v"(s[1]), "v"(s[2]));
            asm("v_max3_f32 %0, %1, %2, %3" : "=v"(n) : "v"(s[3]), "v"(s[4]), "v"(s[5]));
#pragma unroll
            for (int i = 6; i < 14; i += 4) {
                asm("v_max3_f32 %0, %0, %1, %2" : "+v"(m) : "v"(s[i]), "v"(s[i + 1]));
                asm("v_max3_f32 %0, %0, %1, %2" : "+v"(n) : "v"(s[i + 2]), "v"(s[i + 3]));
            }
            asm("v_max3_f32 %0, %0, %1, %2" : "+v"(m) : "v"(s[14]), "v"(s[15]));
            asm("v_max_f32 %0, %0, %1" : "+v"(m) : "v"(n));
            return m;
        };
        auto publish = [&] {
            wait_vm<0>();
            STAMP(13);
            asm volatile("s_mov_b64 exec, %3\n\tglobal_store_dwordx2 %0, %1, %2 sc1\n\ts_mov_b64 exec, -1" :: "v"(fko), "v"(tagv), "s"(fk), "i"((1 << RK) - 1) : "memory");
        };
        {
            f32x16 a0 = {}, a1 = {}, a2 = {};
            bf16x8 xb[4] = {}, wb[4][3] = {};
            ldsptr xp[4], wp = lds + phys(0) + 16384 + (wro ^ hs * 32);
#pragma unroll
            for (int ks = 0; ks < 4; ++ks) xp[ks] = xrd[ks] + phys(0);
            auto mm = [&](int ks, int u) {
                SBAR();
                if (u == 0) { a0 = MFMA(wb[ks][0], xb[ks], a0); PIN(a0); }
                if (u == 1) { a1 = MFMA(wb[ks][1], xb[ks], a1); PIN(a1); }
                if (u == 2) { a2 = MFMA(xb[ks], wb[ks][2], a2); PIN(a2); }
                SBAR();
            };
            auto X = [&](int ks) { xb[ks] = lds_b128(xp[ks]); };
            auto W = [&](int ks, int u) { wb[ks][u] = tr_frag(wp + ks * 3072 + u * 64, wp + ks * 3072 + u * 64 + 768); };
#pragma unroll 1
            for (int s = 0; s < 8; ++s) {
                __builtin_amdgcn_s_waitcnt(0xc07f);
                if (s != 7) asm volatile("s_barrier" ::: "memory");
                STAMP(2 + s);
                mm(2, 0); X(0); W(0, 0);
                mm(2, 1); W(0, 1);
                mm(2, 2); W(0, 2); X(1);
                mm(3, 0); W(1, 0); W(1, 1);
                mm(3, 1); W(1, 2);
                mm(3, 2); X(2); W(2, 1);
                mm(0, 0); W(2, 2); W(2, 0);
                mm(0, 1); X(3); W(3, 1);
                mm(0, 2); W(3, 2);
                int d = s == 2 ? -4 * SLOT : SLOT;
                SPIN(d);
                mm(1, 0); W(3, 0); xp[0] += d; xp[1] += d; PIN(xp[0]); PIN(xp[1]);
                mm(1, 1); xp[2] += d; xp[3] += d; PIN(xp[2]); PIN(xp[3]);
                mm(1, 2); wp += d; PIN(wp);
            }
            __builtin_amdgcn_s_waitcnt(0xc07f);
            set_word(myw, 1);
            mm(2, 1); mm(3, 1); mm(2, 2); mm(3, 2); mm(2, 0); mm(3, 0);
            STAMP(11);
            const bf16x8 k0 = pack8(a1, 0), k1 = pack8(a1, 8);
            gst16_sc1(kvo, ko, k0);
            gst16_sc1(kvo, kp, k1);
            const bf16x8 u0 = pack8(a2, 0), u1 = pack8(a2, 8);
            gst16_sc1(kvo, vo, u0);
            asm volatile("global_store_dwordx4 %0, %1, %2 offset:256 sc1" :: "v"(vo), "v"(u1), "s"(kvo) : "memory");
            STAMP(12);
            SBAR();
            const bf16x8 q1 = pack8(a0, 8, QSCALE);
            sc = MFMA(k1, q1, z);
            PIN(sc);
            SBAR();
            qa = pack8(a0, 0, QSCALE);
            sa = MFMA(k0, qa, z);
            PIN(sa);
            SBAR();
            STAMP(54);
            *(LDSP bf16x8*)(lk0 + 12288) = k0;
            *(LDSP bf16x8*)(lk1 + 12288) = k1;
            *(LDSP bf16x8*)(lv + 12288) = u0;
            *(LDSP bf16x8*)(lv + 12544) = u1;
            *(LDSP bf16x8*)qw = q1;
            *(LDSP unsigned*)(lc + 16384) = cv;
            SBAR();
            STAMP(55);
            if (PE == 3) publish();
            asm volatile("s_nop 5" : "+v"(sc), "+v"(sa));
            float m1 = rowmax(sc), m = rowmax(sa);
            asm volatile("s_nop 1\n\tv_permlane32_swap_b32 %0, %1\n\ts_nop 1\n\tv_min_f32 %0, -%0, -%1" : "+v"(m), "+v"(m1));
            STAMP(56);
            *(LDSP float*)mw = m;
            set_word(myw, two);
            if (PE == 4) publish();
            asm volatile("v_mov_b32 %1, %0\n\ts_nop 1\n\tv_permlane32_swap_b32 %0, %1\n\ts_nop 1" : "+v"(m), "=&v"(m1));
            splat(na, m);
            PIN(na);
            STAMP(10);
            if (PE == 2) publish();
            spin_words<0>(w8, n2);
            STAMP(16);
            kk[0] = lds_b128(krd);
            m1 = *(LDSP float*)mr;
            qc = lds_b128(qr);
            v0[0] = lds_b128(vrd);
            v1[0] = lds_b128(vrd + 256);
            SBAR();
            if (PE == 1) publish();
            SBAR();
            sa = MFMA(kk[0], qa, na);
            splat(nc, m1);
        }
        u32x4 pa[2], pc[2];
        STAMP(19);
        PIN(sa); PIN(nc);
        if (PE == 0) publish();
        auto half = [&](const f32x16& s, u32x4* p, auto m0, auto m1, auto m2, auto l0, auto l1, auto l2) {
            float e[16];
#define EX(i) e[i] = EXP2(s[i])
#define CV(i) p[(i) >> 2][(i) & 3] = cvtpk(e[2 * (i)], e[2 * (i) + 1])
            SBAR(); m0(); SBAR();
            l0();
            SBAR();
            EX(0); EX(1); EX(2);
            asm volatile("" : "+v"(e[0]), "+v"(e[1]), "+v"(e[2]));
            SBAR(); m1(); SBAR();
            EX(3); EX(4); EX(5);
            asm volatile("" : "+v"(e[3]), "+v"(e[4]), "+v"(e[5]));
            SBAR(); m2(); SBAR();
            l1();
            SBAR();
            EX(6); EX(7); SBAR(); CV(0); SBAR();
            EX(8); EX(9); SBAR(); CV(1); SBAR();
            EX(10); EX(11); SBAR(); CV(2); CV(3); SBAR();
            l2();
            SBAR();
            EX(12); EX(13); SBAR(); CV(4); SBAR();
            EX(14); EX(15); SBAR(); CV(5); SBAR(); CV(6); SBAR(); CV(7);
            PIN(p[0]); PIN(p[1]);
            SBAR();
        };
        unsigned so;
        auto kv_addr = [&](unsigned c) {
            asm volatile("s_and_b32 m0, %1, 0x3000\n\ts_xor_b32 %0, %1, %2\n\ts_add_u32 m0, m0, %3" : "=&s"(so) : "s"(c), "s"(sx), "s"(rowb) : "scc");
        };
        auto kv_load = [&](int o) { blds16_sc1(srd, loff, so, o * 1024); };
        auto signal = [&] { asm volatile("ds_sub_u32 %0, %1" :: "v"(cta), "v"(ctv) : "memory"); };
#define BF(x) __builtin_bit_cast(bf16x8, x)
        unsigned c = C0, pv;
        auto step = [&](const int i, const int md, auto mia, auto mid) {
            const int p = i & 1, ro = ((i + 1) & 3) * ROW;
            half(sa, pa, [&] { sc = MFMA(kk[p], qc, nc); }, [&] { if (md != 3) ob = MFMA(v0[p ^ 1], BF(pc[0]), md == 4 ? z : ob); }, [&] { if (md != 3) ob = MFMA(v1[p ^ 1], BF(pc[1]), ob); },
                 [&] { if (i == 2) { pv = get_word((ldsptr)(uintptr_t)cta); SBAR(); } if (md != 2) kk[p ^ 1] = lds_b128(krd + ro); },
                 [&] { if (md != 2) { v0[p ^ 1] = lds_b128(vrd + ro); v1[p ^ 1] = lds_b128(vrd + ro + 256); } },
                 [&] { if (md == 1) kv_load(0); mia(); });
            half(sc, pc, [&] { if (md != 2) sa = MFMA(kk[p ^ 1], qa, na); }, [&] { oa = MFMA(v0[p], BF(pa[0]), md == 3 ? z : oa); }, [&] { oa = MFMA(v1[p], BF(pa[1]), oa); },
                 [&] { if (md == 1) kv_load(1); },
                 [&] { if (md == 1) kv_load(2); mid(); },
                 [&] { if (md == 1) kv_load(3); if (md == 2) { ob = MFMA(v0[p], BF(pc[0]), ob); PIN(ob); } });
        };
        auto none = [] {};
        auto body = [&](auto h0, auto h1a, auto h1d) {
            if (__builtin_expect(__any(pv > c), 0)) spin_count<0>((ldsptr)(uintptr_t)cta, c);
            CFENCE();
            krd = (ldsptr)(uintptr_t)((c & kmask) ^ kbase);
            vrd = (ldsptr)(uintptr_t)((c & vmask) ^ vbase);
            kv_addr(c);
            step(3, 1, none, none);
            c -= 4096;
            step(0, 0, none, h0);
            step(1, 0, h1a, h1d);
            step(2, 0, none, none);
        };
        {
            u64 fv, fw;
            auto poll = [&](u64& f) { asm volatile("global_load_dwordx2 %0, %1, %2 sc1" : "=v"(f) : "v"(flo), "s"(fl) : "memory"); };
            auto loads = [&](unsigned c) {
                kv_addr(c);
                asm volatile("s_nop 0");
                kv_load(0); kv_load(1); kv_load(2); kv_load(3);
            };
            auto first = [&] {
                const bool need = ((lane & 15) ^ rb) - 1u < NF1;
                STAMP(14);
                if (P2) {
                    asm volatile("s_waitcnt vmcnt(1)" : "+v"(fv) :: "memory");
                    if (__builtin_expect(__any(need && fv != tag), 0)) {
                        asm volatile("s_waitcnt vmcnt(0)" : "+v"(fw) :: "memory");
                        if (__any(need && fw != tag)) spin_flags<SLQ>(fl + flo, need, tag);
                    }
                } else {
                    asm volatile("s_waitcnt vmcnt(0)" : "+v"(fv) :: "memory");
                    if (__builtin_expect(__any(need && fv != tag), 0)) spin_flags<SLQ>(fl + flo, need, tag);
                }
                STAMP(17);
                loads(C0 + 8192);
                if (NF1 < 3) poll(fv);
                if (L2P == 0) loads(C0 + 4096);
                STAMP(21);
            };
            STAMP(30);
            step(0, 3, [&] { if (PP == 0) poll(fv); }, [&] { if (PP == 1) poll(fv); });
            STAMP(57);
            step(1, 4, [&] { if (PP == 2) poll(fv); }, [&] { if (PP == 3) poll(fv); if (P2 == 3) poll(fw); });
            STAMP(58);
            step(2, 0, [&] { if (PP == 4) poll(fv); if (P2 == 4) poll(fw); if (PC == 2) first(); }, [&] { if (P2 == 5) poll(fw); if (PC == 3) first(); });
            if (PC == 4) first();
            STAMP(15);
            static_assert(NF1 == 3 || (L2P == 0 ? NF1 == 2 : NF1 >= 1), "flags");
            asm volatile("s_waitcnt vmcnt(%2)" : "+v"(fv), "+v"(fw) : "i"(L2P ? 0 : 4) : "memory");
            signal();
            if (NF1 < 3) {
                const bool need = ((lane & 15) ^ rb) - 1u < 3;
                if (__builtin_expect(__any(need && fv != tag), 0)) spin_flags<SLQ>(fl + flo, need, tag);
            }
            if (L2P) loads(C0 + 4096);
            spin_words<0>(w8, lane & 4 ? 3 : 0);
            STAMP(18);
            body([&] { if (L2P < 2) { wait_vm<4>(); signal(); } }, [&] { if (L2P == 2) { wait_vm<4>(); signal(); } poll(fv); }, [&] { if (L2P == 3) { wait_vm<5>(); signal(); } });
            STAMP(47);
            asm volatile("s_waitcnt vmcnt(0)" : "+v"(fv) :: "memory");
            if (__builtin_expect(__any(fv != tag), 0)) spin_flags<SLQ>(fl + flo, true, tag);
            STAMP(20);
        }
#pragma unroll 1
        do body([&] { wait_vm<4>(); signal(); }, none, none); while (c != C0 - 15 * 4096);
        unsigned mk;
        auto hint = [&] { asm volatile("s_mov_b64 exec, %3\n\tglobal_store_dword %0, %1, %2 sc1\n\ts_mov_b64 exec, -1" :: "v"(hto), "v"((unsigned)tagv), "s"(ws), "s"(hm) : "memory"); };
        if (HP == 0) hint();
        step(3, 2, [&] { mk = get_word(lds + W_CH); if (HP == 1) hint(); }, [&] { if (HP == 2) hint(); });
        ob = MFMA(v1[1], BF(pc[1]), ob);
        PIN(ob); PIN(oa);
        if (HP == 3) hint();
        SBAR();
        STAMP(40);
        auto fin = [&](f32x16& o, bf16x8 qf, char* dst) {
            const float rs = __builtin_amdgcn_rcpf(o[8]);
            f32x2 t0 = f32x2{o[0], o[1]} * rs, t1 = f32x2{o[2], o[3]} * rs, t2 = f32x2{o[4], o[5]} * rs, t3 = f32x2{o[6], o[7]} * rs;
            f32x2 c0, c1;
            u32x4 v;
            u64 bad;
            asm volatile("v_pk_add_f32 %4, %7, %8\n\tv_cvt_pk_bf16_f32 %0, %11, %12\n\tv_pk_add_f32 %5, %9, %10\n\tv_cvt_pk_bf16_f32 %1, %13, %14\n\t"
                         "v_cvt_pk_bf16_f32 %2, %15, %16\n\tv_pk_add_f32 %4, %4, %5\n\tv_cvt_pk_bf16_f32 %3, %17, %18\n\tv_min_u32 %0, -2, %0"
                         : "=&v"(v[0]), "=&v"(v[1]), "=&v"(v[2]), "=&v"(v[3]), "=&v"(c0), "=&v"(c1), "+v"(o)
                         : "v"(t0), "v"(t1), "v"(t2), "v"(t3), "v"(t0[0]), "v"(t0[1]), "v"(t1[0]), "v"(t1[1]), "v"(t2[0]), "v"(t2[1]), "v"(t3[0]), "v"(t3[1]));
            float c2;
            asm volatile("v_fmamk_f32 %3, %5, 0x5f800000, %6\n\tv_min_u32 %0, -2, %0\n\tv_add_f32 %3, %3, %7\n\tv_min_u32 %1, -2, %1\n\tv_cmp_class_f32 %4, %3, %8\n\tv_min_u32 %2, -2, %2"
                         : "+v"(v[1]), "+v"(v[2]), "+v"(v[3]), "=&v"(c2), "=s"(bad) : "v"(o[8]), "v"(c0[0]), "v"(c0[1]), "v"(cls));
            if (__builtin_expect(bad != 0, 0)) {
                const f32x16 e = attn_fallback(ws + WS_KV + (size_t)cg * 262144 + hs * 1024, qf, lane);
                v = __builtin_bit_cast(u32x4, pack8(e, 0, __builtin_amdgcn_rcpf(e[8])));
#pragma unroll
                for (int i = 0; i < 4; ++i) v[i] = min(v[i], ~1u);
            }
            asm volatile("global_store_dwordx4 %0, %1, %2 sc1" :: "v"(loff), "v"(v), "s"(dst) : "memory");
            return v;
        };
        check_words(mk, lds + W_CH, 4);
        u32x4 keep = fin(oa, qa, opa);
        PIN(ob);
        if (HP == 4) hint();
        fin(ob, qc, opb);
        PIN(keep);
        STAMP(52);
        wait_vm<0>();
        STAMP(53);
        asm volatile(".Lcode_end1:");
    } else {
        typedef __attribute__((ext_vector_type(8))) u64 u64x8;
        u64x8 ka;
        u64 kb;
        asm volatile("s_load_dwordx16 %0, %2, 0\n\ts_load_dwordx2 %1, %2, 0x40" : "=&s"(ka), "=&s"(kb) : "s"(__builtin_amdgcn_kernarg_segment_ptr()));
        GEOMETRY
        {
            unsigned len;
            asm volatile("s_mov_b32 %0, .Lcode_end0-.Lcode_begin-4\n\ts_max_u32 %0, %0, .Lcode_end1-.Lcode_begin-4" : "=s"(len) :: "scc");
            const unsigned line = min((unsigned)(((idx % 3) * 128 + tt * 32 + r) * 128), len);
            asm volatile("s_mov_b32 m0, %2\n\ts_nop 0\n\tglobal_load_lds_dword %0, %1" :: "v"(line), "s"(pc), "i"(DUMP) : "memory");
        }
        SBAR();
        const int mb = xcd * 4 + (idx & 3), nb = idx >> 2;
        const int rot = (idx & 7) + 2 * (idx >> 3);
        unsigned xoff[2], woff[3], foff[3];
#pragma unroll
        for (int e = 0; e < 2; ++e) {
            const int row = tt * 32 + e * 8 + (lane >> 3);
            xoff[e] = (unsigned)((rb * 128 + row) * 1024 + (((lane & 7) ^ ((row >> 1) & 7)) * 16));
        }
        const int u0 = cg < 14 ? cg * 6 : 80;
#pragma unroll
        for (int e = 0; e < 3; ++e) {
            const int n = (tt * 3 + e) * 64 + lane, kr = n / 12, c = n % 12;
            woff[e] = (unsigned)(kr * 3072 + cg * 64 + (c >> 2) * (1024 - 64) + c * 16);
            foff[e] = (unsigned)(kr * (F_ * 2) + u0 * 32 + c * 16);
        }
        unsigned ldx = tt * 4096, ldw = tt * 3072, ldr = tt * ROW;
        const ldsptr myw = lds + W_PF + tt * 4;
        SPIN(ldx); SPIN(ldw); SPIN(ldr);
        int kbs[8];
#pragma unroll
        for (int i = 0; i < 8; ++i) { kbs[i] = (i + rot) & 7; SPIN(kbs[i]); }
        PIN(xoff[0]); PIN(xoff[1]); PIN(woff[0]); PIN(woff[1]); PIN(woff[2]); PIN(foff[0]); PIN(foff[1]); PIN(foff[2]);
        asm volatile("s_waitcnt lgkmcnt(0)" : "+s"(ka), "+s"(kb));
        STAMP(59);
        char* ws = (char*)ka[7];
        asm volatile("s_load_dwordx2 %0, %1, 0" : "=&s"(ep) : "s"(ws + WS_EP));
        const u64 x2 = ka[0] + 16384;
        unsigned xlo = ka[0], xhi = ka[0] >> 32, ylo = x2, yhi = x2 >> 32, wlo = ka[1], whi = ka[1] >> 32;
        unsigned glo = ka[3], ghi = ka[3] >> 32, ulo = ka[4], uhi = ka[4] >> 32;
        SPIN(xlo); SPIN(xhi); SPIN(ylo); SPIN(yhi); SPIN(wlo); SPIN(whi); SPIN(glo); SPIN(ghi); SPIN(ulo); SPIN(uhi);
        auto issue = [&](auto n_) {
            constexpr int n = decltype(n_)::value;
            if constexpr (n < 56) {
                constexpr int st = n / 7, op = n % 7, e = op >> 1;
                if (op & 1) glds16k(wlo, whi, kbs[st], 64 * 3072, woff[e], ldw, phys(st) + 16384 + e * 1024);
                else glds16k(e < 2 ? xlo : ylo, e < 2 ? xhi : yhi, kbs[st], 128, xoff[e & 1], ldx, phys(st) + e * 1024);
            } else {
                constexpr int hi = (n - 56) / 3, e = (n - 56) % 3;
                glds16k(hi < 8 ? glo : ulo, hi < 8 ? ghi : uhi, kbs[hi & 7], 64 * F_ * 2, foff[e], hi < 1 ? ldr : ldw, hphys(hi) + e * 1024);
            }
        };
        sfor<0, cum(0)>(issue);
        STAMP(1);
        asm volatile("s_waitcnt lgkmcnt(0)" : "+s"(ep));
        const u64 tag = (ep ^ (ep >> 31)) * 0x9E3779B97F4A7C15ull + 0xD1B54A32D192ED03ull;
        bf16x8 xr[32];
        sfor<0, 7>([&](auto s_) {
            constexpr int s = decltype(s_)::value, e = s == 6 ? 7 : s;
            static_assert(cum(s) >= endi(e) && cum(5) == 56 && K6 <= 3 && CAPQ <= 65 && F0 <= (GA ? 65 : 77), "stage not issued");
            wait_vm<cum(s) - endi(e)>();
            bar();
            STAMP(2 + s);
#pragma unroll
            for (int ks = 4 * s; ks < 4 * e + 4; ++ks) xr[ks] = lds_b128(ladd(xrd[ks & 3], phys(ks >> 2)));
            SBAR();
            sfor<cum(s), cum(s + 1)>(issue);
        });
        ldsptr wp4 = lds + W_PF + p4 * 4;
        if (cg == 15) {
            glds16((const char*)ka[6], (loff + rot * 128) & 1023, tt * 1024, GATE);
            wait_vm<0>();
            asm volatile("s_waitcnt lgkmcnt(0)" ::: "memory");
            set_word(myw, 17);
            f32x16 ag = {};
            const ldsptr gp = lds + GATE + tt * 1024 + h * 16;
#pragma unroll
            for (int g = 0; g < 32; ++g) ag = MFMA(lds_b128(gp + g * 32), xr[g], ag);
            gst4_sc1(ws + WS_G + T * 128, r * 4, ag[0]);
            set_word(myw, 18);
        } else {
            wait_vm<CAPQ - endh(0)>();
            asm volatile("s_waitcnt lgkmcnt(0)" ::: "memory");
            set_word(myw, 1);
            STAMP(10);
            spin_words<0>(GA ? wp4 : lds + W_PA + (lane & 7) * 4, 1);
            STAMP(11);
            sfor<CAPQ, cumf(0)>(issue);
            __builtin_amdgcn_s_setprio(PF1);
            bf16x8 fw[3][3], sg[6];
            f32x16 fa[3];
            ldsptr frd = lds + wro, fs_;
            auto frd_ = [&](int g, int u) {
                if ((g & 3) == 0 && u == 0) fs_ = ladd(frd, hphys(g >> 2));
                const int so = (g & 3) * (g < 4 ? ROW : 3072) + u * 64;
                fw[g % 3][u] = tr_frag(fs_ + so, fs_ + so + 768);
            };
#pragma unroll
            for (int n = 0; n < 6; ++n) frd_(n / 3, n % 3);
            unsigned pv;
            sfor<0, 64>([&](auto g_) {
                constexpr int g = decltype(g_)::value;
                if constexpr (g == 32) {
                    STAMP(14);
                    __builtin_amdgcn_s_setprio(PSI);
#pragma unroll
                    for (int n = 0; n < 6; ++n) {
                        const int u = n >> 1, o = (n & 1) * 8;
                        f32x2 e[4];
                        SBAR();
#pragma unroll
                        for (int j = 0; j < 4; ++j) e[j] = -LOG2E * f32x2{fa[u][o + 2 * j], fa[u][o + 2 * j + 1]};
                        SBAR();
#pragma unroll
                        for (int j = 0; j < 8; ++j) e[j >> 1][j & 1] = EXP2(e[j >> 1][j & 1]);
                        SBAR();
#pragma unroll
                        for (int j = 0; j < 4; ++j) e[j] += 1.0f;
                        SBAR();
#pragma unroll
                        for (int j = 0; j < 8; ++j) e[j >> 1][j & 1] = __builtin_amdgcn_rcpf(e[j >> 1][j & 1]);
                        SBAR();
#pragma unroll
                        for (int j = 0; j < 4; ++j) e[j] *= f32x2{fa[u][o + 2 * j], fa[u][o + 2 * j + 1]};
                        SBAR();
                        u32x4 v = {cvtpk(e[0][0], e[0][1]), cvtpk(e[1][0], e[1][1]), cvtpk(e[2][0], e[2][1]), cvtpk(e[3][0], e[3][1])};
                        sg[n] = __builtin_bit_cast(bf16x8, v);
                        PIN(sg[n]);
                    }
                    __builtin_amdgcn_s_setprio(PFU);
                    STAMP(15);
                }
                if constexpr ((g & 3) == 2 && g < 60) check_words(pv, wp4, ((g + 2) >> 2) + 1);
                if constexpr (g == GA && GA) spin_words<0>(wp4 - 16, 1);
#pragma unroll
                for (int u = 0; u < 3; ++u) {
                    SBAR();
                    if ((g & 31) == 0) { const f32x16 z = {}; fa[u] = MFMA(fw[g % 3][u], xr[g & 31], z); }
                    else fa[u] = MFMA(fw[g % 3][u], xr[g & 31], fa[u]);
                    PIN(fa[u]);
                    SBAR();
                    if constexpr ((g & 3) == 0 && g < 60) {
                        if (u == 0) {
                            constexpr int hi = (g >> 2) + 1;
                            static_assert(cumf(g) >= endh(hi), "block not issued");
                            wait_vm<cumf(g) - endh(hi)>();
                            set_word(myw, hi + 1);
                            if (g == 12) STAMP(12);
                            if (g == 16) STAMP(13);
                            if (g == 4) STAMP(16);
                            if (g == 8) STAMP(17);
                        }
                    }
                    if ((g & 3) == 1 && g < 60 && u == 1) pv = get_word(wp4);
                    if (g < 62) frd_(g + 2, u);
                    if (u == 0) sfor<cumf(g), cumf(g + 1)>(issue);
                }
                SBAR();
            });
            __builtin_amdgcn_s_setprio(PRF);
            set_word(myw, 18);
            STAMP(20);
#pragma unroll
            for (int n = 0; n < 6; ++n) {
                const int u = n >> 1, o = (n & 1) * 8;
                const u32x4 s4 = __builtin_bit_cast(u32x4, sg[n]);
                u32x4 v;
#pragma unroll
                for (int j = 0; j < 4; ++j)
                    v[j] = cvtpk(__builtin_bit_cast(float, s4[j] << 16) * fa[u][o + 2 * j], __builtin_bit_cast(float, s4[j] & 0xffff0000u) * fa[u][o + 2 * j + 1]);
                const bool own = (cg < 14) | (n >= 4);
                const unsigned a = own ? WS_H + (T * NKF + u0 + n) * 1024 : WS_DUMP + (blockIdx.x * 4 + tt) * 1024;
                gst16_sc1(ws + a, loff, __builtin_bit_cast(bf16x8, v));
            }
        }
        {
            const u32x4 o = {~0u, ~0u, ~0u, ~0u};
            char* op = ws + WS_O + (size_t)(T * NH + 2 * cg) * 1024;
            gst16_sc1(op, loff, __builtin_bit_cast(bf16x8, o));
            gst16_sc1(op + 1024, loff, __builtin_bit_cast(bf16x8, o));
        }
        STAMP(21);
        const int mh = tt & 1, nh = tt >> 1;
        const int fk0 = tt * 21 + (tt < 2 ? tt : 2);
        const char* hfa = ws + WS_H + ((size_t)(2 * mb + mh) * NKF + fk0) * 1024;
        const char* hfb = ws + WS_H + ((size_t)(2 * mb + 1 - mh) * NKF + fk0) * 1024;
        const char* wdp = (const char*)ka[5] + (size_t)fk0 * 16384;
        const char* wop = (const char*)ka[2] + tt * 131072;
        const int drow = lane >> 3;
        const unsigned doff = (unsigned)(drow * 1024 + nb * 128 + (((lane & 7) ^ (((drow >> 1) & 1) << 2)) * 16));
        const int row0 = 4 * h + q, sw = (row0 >> 1) & 1;
        const int wfo = (row0 * 128 + sw * 64 + (p4 & 1) * 32 + blk * 16 + (p4 >> 1) * 8) ^ (nh * 64);
        unsigned regb = WB + tt * REG;
        SPIN(regb);
        const int last = tt < 2 ? 21 : 20;
        auto load_w = [&](const char* p, unsigned b, int slot) {
            glds16(p, doff, b, slot * 2048);
            glds16(p + 8192, doff, b, slot * 2048 + 1024);
        };
        const ldsptr cta = lds + W_CT + r * 4;
        wait_vm<0>();
        const bool lasth = arrive_last(W_CH);
        if (lasth && lane < 4) __hip_atomic_store((gu64*)(ws + WS_FH + lane * 2048 + rb * 128 + cg * 8), tag, RLX_AGENT);
        STAMP(23);
        __builtin_amdgcn_s_setprio(PRM);
        spin_words<1>(wp4, 18);
        spin_count<4>(cta, C0);
        STAMP(22);
        auto warm = [&] {
            const int w = (idx * 4 + tt + xcd * 16) & 127;
            const char* p = (const char*)ka[5] + w * (86 * 128);
            if (PG & 1) glds4(p, lane * 128, regb, 10 * 2048);
            if (PG & 1) glds4(p + 8192, min(lane, 21) * 128, regb, 10 * 2048);
            if (PG & 2) glds4((const char*)ka[5] + ((w >> 5) * 21 + min(w >> 5, 2) + 11) * 16384 + (w & 31) * 5120, min(lane, 39) * 128, regb, 10 * 2048);
            if (PG & 3) glds4((const char*)ka[2] + w * 4096, r * 128, regb, 10 * 2048);
        };
        if (!(PG & 4)) warm();
#pragma unroll
        for (int n = 0; n < 11; ++n) { if (n == 10 && (PG & 4)) warm(); load_w(wdp + n * 16384, regb, n); if (WS) __builtin_amdgcn_s_sleep(WS); }
        STAMP(24);
        if (lasth) {
            spin_flags<8>(ws + WS_FH + (nb & 3) * 2048 + (mb >> 1) * 128 + (lane & 15) * 8, true, tag);
            set_word(lds + W_GH, 1);
        } else spin_words<4>(lds + W_GH, 1);
        STAMP(25);
        f32x16 acc[4] = {};
        bf16x8 ha[HD], hb[HD];
        float tgate;
        const char *of = ws + WS_O + (size_t)((2 * mb + mh) * NH + nh * 16) * 1024, *hb_ = ws + WS_HT + mb * 256 + nh * 128;
        unsigned po = (lane & 7) * 4, pr, fly;
        f32x16 ov[4];
        auto probe0 = [&] { asm volatile("global_load_dword %0, %1, %2 sc1" : "=&{v194}"(pr) : "v"(po), "s"(hb_) : "memory"); };
        bf16x8 w7a, w7b;
        auto load_w7 = [&] {
            const char* p = (const char*)ka[2] + ((tt ^ 1) * 8 + 7) * 16384;
            asm volatile("global_load_dwordx4 %0, %2, %3\n\tglobal_load_dwordx4 %1, %2, %4" : "=&v"(w7a), "=&v"(w7b) : "v"(doff), "s"(p), "s"(p + 8192) : "memory");
        };
        auto load_h = [&](int n) {
            const int o = (n < 21 ? n : last) * 1024;
            gld16_sc1(ha[n % HD], hfa + o, loff);
            gld16_sc1(hb[n % HD], hfb + o, loff);
        };
        asm volatile("global_load_dword %0, %1, %2 sc1" : "=v"(tgate) : "v"(r * 4), "s"(ws + WS_G + (2 * mb + mh) * 128) : "memory");
#pragma unroll
        for (int n = 0; n < HD; ++n) load_h(n);
        ldsptr wr0 = lds + WB + tt * REG + wfo, wr1 = lds + WB + tt * REG + (wfo ^ 64);
        sfor<0, 22>([&](auto n_) {
            constexpr int n = decltype(n_)::value, e = n % HD, so = n % 11 * 2048;
            wait_vm2<f2wait(n)>(ha[e], hb[e]);
            if (n < 21 || tt < 2) {
                const bf16x8 w0 = tr_frag(wr0 + so, wr0 + so + 1024), w1 = tr_frag(wr1 + so, wr1 + so + 1024);
                acc[0] = MFMA(w0, ha[e], acc[0]);
                acc[2] = MFMA(w0, hb[e], acc[2]);
                acc[1] = MFMA(w1, ha[e], acc[1]);
                acc[3] = MFMA(w1, hb[e], acc[3]);
                PIN(acc[0]); PIN(acc[1]); PIN(acc[2]); PIN(acc[3]);
            }
            SBAR();
            if (n <= 10) load_w(wdp + (n < 10 ? n + 11 : last) * 16384, regb, n);
            else if (n <= 17) load_w(wop + (n - 11) * 16384, regb, n - 11);
            else if (n == 18) load_w7();
            if (n + HD < 22) load_h(n + HD);
        });
        asm volatile("" : "+v"(tgate));
        __builtin_amdgcn_s_setprio(PRX);
        STAMP(26);
        if (PB2 == 4) probe0();
        LDSP f32x4* xch = (LDSP f32x4*)(lds + WB + 14336) + lane;
        constexpr int RG = REG / 16;
#pragma unroll
        for (int i = 0; i < 8; ++i) xch[tt * RG + i * 64] = f32x4{acc[2 + i / 4][4 * (i & 3)], acc[2 + i / 4][4 * (i & 3) + 1], acc[2 + i / 4][4 * (i & 3) + 2], acc[2 + i / 4][4 * (i & 3) + 3]};
        set_word(myw, 20);
        spin_words<XS>(X1 ? lds + W_PF + (tt ^ 1) * 4 : wp4, 20);
#pragma unroll
        for (int i = 0; i < 8; ++i) {
            const f32x4 v = xch[(tt ^ 1) * RG + i * 64];
#pragma unroll
            for (int j = 0; j < 4; ++j) acc[i / 4][4 * (i & 3) + j] += v[j];
        }
        PIN(acc[0]); PIN(acc[1]);
        CFENCE();
        wait_vm2<PB2 == 4 ? 1 : 0>(w7a, w7b);
        if (PB2 == 0) probe0();
        xch[(tt ^ 1) * RG] = __builtin_bit_cast(f32x4, w7a);
        xch[(tt ^ 1) * RG + 64] = __builtin_bit_cast(f32x4, w7b);
#pragma unroll
        for (int i = 0; i < 4; ++i) xch[(tt ^ 1) * RG + (i + 2) * 64] = f32x4{acc[1][4 * i], acc[1][4 * i + 1], acc[1][4 * i + 2], acc[1][4 * i + 3]};
        set_word(myw, 22);
        asm volatile(".irp i,64,72,80,88,96,104,112,120\n\tv_mov_b64 v[\\i:\\i+1], -1\n\tv_mov_b64 v[\\i+2:\\i+3], -1\n\tv_mov_b64 v[\\i+4:\\i+5], -1\n\tv_mov_b64 v[\\i+6:\\i+7], -1\n\t.endr"
                     : "={v[64:79]}"(ov[0]), "={v[80:95]}"(ov[1]), "={v[96:111]}"(ov[2]), "={v[112:127]}"(ov[3]));
        spin_words<XS>(wp4, 22);
        const float ga = __builtin_amdgcn_rcpf(1.0f + EXP2(-LOG2E * tgate)), og = 1.0f - ga;
        f32x16 bs;
#pragma unroll
        for (int i = 0; i < 4; ++i) {
            const f32x4 v = xch[(tt ^ 3) * RG + (i + 2) * 64];
#pragma unroll
            for (int j = 0; j < 4; ++j) bs[4 * i + j] = (acc[0][4 * i + j] + v[j]) * og;
        }
        PIN(bs);
        CFENCE();
        STAMP(27);
        f32x16 a[2][2];
        ldsptr wq0 = lds + WB + 2 * nh * REG + wfo, wq1 = lds + WB + 2 * nh * REG + (wfo ^ 64);
        char* out = (char*)kb;
        unsigned oo = (mb * 64 + mh * 32 + r) * 1024 + (nb * 64 + nh * 32 + h * 16) * 2;
        oo = (mb * 64 + mh * 32 + (lane & 15)) * 1024 + (nb * 64 + nh * 32) * 2 + (lane >> 4) * 16;
        LDSP f32x4* xw = xch + (tt ^ 3) * RG + 128;
        LDSP f32x4* xr2 = xch + (tt ^ 1) * RG + 128;
        ldsptr pwd = lds + W_PF + (tt ^ 2) * 4;
        ldsptr myv = myw;
        unsigned w26 = 26;
        char* out2 = out + 16384;
        const char *ob0 = of + 4096, *ob1 = of + 12288;
        PIN(wq0); PIN(wq1); SPIN(ob0); SPIN(ob1);
        PIN(oo); PIN(xw); PIN(xr2); PIN(pwd); PIN(myv); PIN(w26); SPIN(out2);
        spin_count<8>(cta, C0 - PS * 4096);
        if (PB2 == 3) probe0();
        __builtin_amdgcn_s_setprio(PRT);
        STAMP(41);
        {
            unsigned pend, val, was, cnt, req, lim;
            asm volatile(
                ".macro RDW c, j, e\n\t"
                "ds_read_b64_tr_b16 v[128+32*\\c+16*(\\j&1)+8*\\e:129+32*\\c+16*(\\j&1)+8*\\e], %[w0] offset:%[rg]*\\c+4096*\\j+2048*\\e\n\t"
                "ds_read_b64_tr_b16 v[130+32*\\c+16*(\\j&1)+8*\\e:131+32*\\c+16*(\\j&1)+8*\\e], %[w0] offset:%[rg]*\\c+4096*\\j+2048*\\e+1024\n\t"
                "ds_read_b64_tr_b16 v[132+32*\\c+16*(\\j&1)+8*\\e:133+32*\\c+16*(\\j&1)+8*\\e], %[w1] offset:%[rg]*\\c+4096*\\j+2048*\\e\n\t"
                "ds_read_b64_tr_b16 v[134+32*\\c+16*(\\j&1)+8*\\e:135+32*\\c+16*(\\j&1)+8*\\e], %[w1] offset:%[rg]*\\c+4096*\\j+2048*\\e+1024\n\t"
                ".endm\n\t"
                ".macro MM c, j, n\n\t"
                ".if (\\j == 0) && (\\n < 2)\n\t"
                "v_mfma_f32_32x32x16_bf16 v[32*\\c+16*(\\n&1):32*\\c+16*(\\n&1)+15], v[128+32*\\c+16*(\\j&1)+4*\\n:131+32*\\c+16*(\\j&1)+4*\\n], v[64+32*\\c+8*\\j+4*(\\n>>1):67+32*\\c+8*\\j+4*(\\n>>1)], 0\n\t"
                ".else\n\t"
                "v_mfma_f32_32x32x16_bf16 v[32*\\c+16*(\\n&1):32*\\c+16*(\\n&1)+15], v[128+32*\\c+16*(\\j&1)+4*\\n:131+32*\\c+16*(\\j&1)+4*\\n], v[64+32*\\c+8*\\j+4*(\\n>>1):67+32*\\c+8*\\j+4*(\\n>>1)], v[32*\\c+16*(\\n&1):32*\\c+16*(\\n&1)+15]\n\t"
                ".endif\n\t"
                ".endm\n\t"
                ".macro LD u, j, base\n\t"
                "global_load_dwordx4 v[64+8*\\u:67+8*\\u], %[lo], \\base offset:2048*\\j-4096 sc1\n\t"
                "global_load_dwordx4 v[68+8*\\u:71+8*\\u], %[lo], \\base offset:2048*\\j-3072 sc1\n\t"
                ".endm\n\t"
                ".macro NEW u, j, base, x\n\t"
                "s_bitcmp1_b32 %[fly], \\u\n\t"
                "s_cbranch_scc0 .Ln\\x\\u\n\t"
                "LD \\u, \\j, \\base\n"
                ".Ln\\x\\u:\n\t"
                ".endm\n\t"
                ".macro NEW8 x\n\t"
                "NEW 0, 0, %[b0], \\x\n\tNEW 1, 1, %[b0], \\x\n\tNEW 2, 2, %[b0], \\x\n\tNEW 3, 3, %[b0], \\x\n\t"
                "NEW 4, 0, %[b1], \\x\n\tNEW 5, 1, %[b1], \\x\n\tNEW 6, 2, %[b1], \\x\n\tNEW 7, 3, %[b1], \\x\n\t"
                ".endm\n\t"
                ".macro UNIT u, c, j, base, nx\n\t"
                ".p2align 8\n\t"
                "v_max3_u32 v192, v[64+8*\\u], v[65+8*\\u], v[66+8*\\u]\n\t"
                "v_max3_u32 v193, v[67+8*\\u], v[68+8*\\u], v[69+8*\\u]\n\t"
                "v_max3_u32 v192, v192, v[70+8*\\u], v[71+8*\\u]\n\t"
                "v_max_u32 v192, v192, v193\n\t"
                "v_cmp_lt_u32 vcc, %[lim], v192\n\t"
                "s_cbranch_vccnz .Lk\\u\n\t"
                ".if \\j > 0\n\t"
                "s_bitset1_b32 %[val], \\u\n\t"
                "s_bitcmp1_b32 %[pend], \\u-1\n\t"
                "s_cbranch_scc1 .Lchk\n\t"
                ".endif\n"
                ".Lp\\u:\n\t"
                ".if \\j == 2\n\t"
                "s_waitcnt lgkmcnt(8)\n\t"
                ".endif\n\t"
                ".if \\j == 3\n\t"
                "s_waitcnt lgkmcnt(0)\n\t"
                ".endif\n\t"
                "MM \\c, \\j, 1\n\t"
                "s_bitset0_b32 %[pend], \\u\n\t"
                ".if \\u < 7\n\t"
                "v_max3_u32 v192, v[72+8*\\u], v[73+8*\\u], v[74+8*\\u]\n\t"
                "v_max3_u32 v193, v[75+8*\\u], v[76+8*\\u], v[77+8*\\u]\n\t"
                ".endif\n\t"
                "s_and_b32 %[was], %[was], %[pend]\n\t"
                "MM \\c, \\j, 0\n\t"
                ".if \\u < 7\n\t"
                "v_max3_u32 v192, v192, v[78+8*\\u], v[79+8*\\u]\n\t"
                ".endif\n\t"
                "s_and_b32 %[fly], %[fly], %[pend]\n\t"
                ".if \\j < 2\n\t"
                "RDW \\c, (\\j+2), 0\n\t"
                ".endif\n\t"
                ".if \\u < 7\n\t"
                "v_max_u32 v192, v192, v193\n\t"
                ".endif\n\t"
                "MM \\c, \\j, 3\n\t"
                ".if \\u < 7\n\t"
                "v_cmp_eq_u32 vcc, -1, v192\n\t"
                ".endif\n\t"
                ".if \\u == 3\n\t"
                "s_bitcmp1_b32 %[pend], 4\n\t"
                "s_cselect_b64 vcc, vcc, -1\n\t"
                ".endif\n\t"
                ".if \\j == 3\n\t"
                "s_cmp_eq_u32 %[pend], 0\n\t"
                ".else\n\t"
                "s_bitcmp1_b32 %[val], \\u+1\n\t"
                ".endif\n\t"
                "MM \\c, \\j, 2\n\t"
                ".if \\j < 2\n\t"
                "RDW \\c, (\\j+2), 1\n\t"
                ".endif\n\t"
                ".if \\u < 7\n\t"
                "s_cbranch_vccz .Lp\\nx\n\t"
                ".endif\n\t"
                ".if \\j < 3\n\t"
                "s_cbranch_scc1 .Lp\\nx\n\t"
                ".else\n\t"
                "s_cbranch_scc1 .Ldone\n\t"
                ".endif\n\t"
                "s_branch .Lchk\n"
                ".Lk\\u:\n\t"
                "LD \\u, \\j, \\base\n\t"
                "s_bitset1_b32 %[fly], \\u\n\t"
                "s_branch .Lchk\n\t"
                ".endm\n\t"
                "s_waitcnt vmcnt(0)\n\t"
                "v_cmp_eq_u32 vcc, %[tg], v194\n\t"
                "s_and_b32 %[fly], vcc_lo, 0xff\n\t"
                "NEW8 a\n\t"
                "s_cmp_eq_u32 %[fly], 0xff\n\t"
                "s_cbranch_scc1 .Lall\n\t"
                "global_load_dword v194, %[po], %[hb] sc1\n"
                ".Lall:\n\t"
                ".irp e,0,1\n\tRDW 0, 0, \\e\n\tRDW 0, 1, \\e\n\tRDW 1, 0, \\e\n\tRDW 1, 1, \\e\n\t.endr\n\t"
                "s_getpc_b64 s[90:91]\n"
                ".Lpc:\n\t"
                "s_add_u32 s90, s90, .Ltab-.Lpc\n\t"
                "s_addc_u32 s91, s91, 0\n\t"
                "s_movk_i32 %[pend], 0xff\n\t"
                "s_mov_b32 %[val], 0\n\t"
                "s_mov_b32 %[was], 0\n\t"
                "s_or_b32 %[req], %[fly], ~0xff\n\t"
                "s_mov_b32 %[lim], -2\n\t"
                "s_mov_b32 %[cnt], 0x100000\n\t"
                "s_waitcnt lgkmcnt(0)\n"
                ".Lloop:\n\t"
                "s_cmp_eq_u32 %[fly], 0xff\n\t"
                "s_cbranch_scc0 .Lslow\n\t"
                "s_waitcnt vmcnt(%[fw])\n\t"
                "v_max3_u32 v192, v64, v65, v66\n\t"
                "v_max3_u32 v193, v67, v68, v69\n\t"
                "v_max3_u32 v192, v192, v70, v71\n\t"
                "v_max_u32 v192, v192, v193\n\t"
                "v_cmp_eq_u32 vcc, -1, v192\n\t"
                "s_cbranch_vccz .Lp0\n"
                ".Lslow:\n\t"
                "s_mov_b32 %[was], %[fly]\n\t"
                "s_mov_b32 %[fly], 0\n\t"
                "s_cmp_eq_u32 %[req], -1\n\t"
                "s_waitcnt vmcnt(0)\n\t"
                "s_cbranch_scc0 .Lprobe\n"
                ".Lchk:\n\t"
                "s_ff1_i32_b32 s92, %[was]\n\t"
                "s_cmp_eq_u32 %[was], 0\n\t"
                "s_cbranch_scc1 .Lidle\n\t"
                "s_bitset0_b32 %[was], s92\n\t"
                "s_lshl_b32 s92, s92, 8\n\t"
                "s_add_u32 s92, s92, s90\n\t"
                "s_addc_u32 s93, s91, 0\n\t"
                "s_setpc_b64 s[92:93]\n"
                ".Lidle:\n\t"
                "s_sub_u32 %[cnt], %[cnt], 1\n\t"
                "s_cselect_b32 %[lim], -1, %[lim]\n\t"
                "s_cmp_eq_u32 %[req], -1\n\t"
                "s_cbranch_scc1 .Lloop\n\t"
                "s_sleep %[sl]\n\t"
                "global_load_dword v194, %[po], %[hb] sc1\n\t"
                "s_branch .Lloop\n"
                ".Lprobe:\n\t"
                "v_cmp_eq_u32 vcc, %[tg], v194\n\t"
                "s_cmp_eq_u32 %[lim], -1\n\t"
                "s_cselect_b32 %[fly], -1, vcc_lo\n\t"
                "s_andn2_b32 %[fly], %[fly], %[req]\n"
                "\ts_cbranch_scc0 .Lchk\n\t"
                "s_or_b32 %[req], %[req], %[fly]\n\t"
                "NEW8 b\n\t"
                "s_branch .Lchk\n\t"
                ".p2align 8\n"
                ".Ltab:\n\t"
                "UNIT 0, 0, 0, %[b0], 1\n\tUNIT 1, 0, 1, %[b0], 2\n\tUNIT 2, 0, 2, %[b0], 3\n\tUNIT 3, 0, 3, %[b0], 4\n\t"
                "UNIT 4, 1, 0, %[b1], 5\n\tUNIT 5, 1, 1, %[b1], 6\n\tUNIT 6, 1, 2, %[b1], 7\n\tUNIT 7, 1, 3, %[b1], 8\n"
                ".Ldone:\n\t"
                "s_waitcnt vmcnt(0)\n\t"
                "s_nop 10"
                : "=&{v[0:15]}"(a[0][0]), "=&{v[16:31]}"(a[0][1]), "=&{v[32:47]}"(a[1][0]), "=&{v[48:63]}"(a[1][1]),
                  "+{v[64:79]}"(ov[0]), "+{v[80:95]}"(ov[1]), "+{v[96:111]}"(ov[2]), "+{v[112:127]}"(ov[3]), "+{v194}"(pr),
                  [pend] "=&s"(pend), [fly] "=&s"(fly), [val] "=&s"(val), [was] "=&s"(was), [cnt] "=&s"(cnt), [req] "=&s"(req), [lim] "=&s"(lim)
                : [w0] "v"(wq0), [w1] "v"(wq1), [lo] "v"(loff), [po] "v"(po), [b0] "s"(ob0), [b1] "s"(ob1), [hb] "s"(hb_), [tg] "s"((unsigned)tag),
                  [rg] "i"(REG), [sl] "i"(SLO), [fw] "i"(FW)
                : "memory", "scc", "vcc", "s90", "s91", "s92", "s93", CLOB
                );
        }
        STAMP(42);
        a[0][1] += a[1][1];
#pragma unroll
        for (int i = 0; i < 4; ++i) xw[i * 64] = f32x4{a[0][1][4 * i], a[0][1][4 * i + 1], a[0][1][4 * i + 2], a[0][1][4 * i + 3]};
        set_word(myv, w26);
        SBAR();
        f32x16 af;
        {
            unsigned w, cnt = 1 << 22;
            f32x4 v[4];
#define RD5 "ds_read_b32 %0, %6\n\tds_read_b128 %1, %7\n\tds_read_b128 %2, %7 offset:1024\n\tds_read_b128 %3, %7 offset:2048\n\tds_read_b128 %4, %7 offset:3072\n\t"
            asm volatile(RD5 : "=&v"(w), "=&v"(v[0]), "=&v"(v[1]), "=&v"(v[2]), "=&v"(v[3]), "+s"(cnt) : "v"(pwd), "v"(xr2) : "memory");
            SBAR();
            af = (a[0][0] + a[1][0]) * ga + bs;
            PIN(af);
            SBAR();
            asm volatile("s_waitcnt lgkmcnt(4)\n\tv_cmp_gt_u32 vcc, 26, %0\n\ts_cbranch_vccz 2f\n1:\n\t" RD5
                         "s_sub_u32 %5, %5, 1\n\ts_cbranch_scc1 2f\n\ts_waitcnt lgkmcnt(4)\n\tv_cmp_gt_u32 vcc, 26, %0\n\ts_cbranch_vccnz 1b\n2:\n\ts_waitcnt lgkmcnt(0)"
                         : "+v"(w), "+v"(v[0]), "+v"(v[1]), "+v"(v[2]), "+v"(v[3]), "+s"(cnt) : "v"(pwd), "v"(xr2) : "memory", "scc", "vcc");
#pragma unroll
            for (int i = 0; i < 16; ++i) af[i] = v[i >> 2][i & 3] * ga + af[i];
            PIN(af);
        }
        STAMP(43);
        {
            const bf16x8 o0 = pack8(af, 0), o1 = pack8(af, 8);
            {
                u32x4 p0 = __builtin_bit_cast(u32x4, o0), p1 = __builtin_bit_cast(u32x4, o1);
                asm volatile("s_nop 1\n\tv_permlane16_swap_b32 %0, %4\n\tv_permlane16_swap_b32 %1, %5\n\tv_permlane16_swap_b32 %2, %6\n\tv_permlane16_swap_b32 %3, %7\n\ts_nop 1"
                             : "+v"(p0[0]), "+v"(p0[1]), "+v"(p0[2]), "+v"(p0[3]), "+v"(p1[0]), "+v"(p1[1]), "+v"(p1[2]), "+v"(p1[3]));
                asm volatile("global_store_dwordx4 %0, %1, %3" OM "\n\tglobal_store_dwordx4 %0, %2, %4" OM :: "v"(oo), "v"(p0), "v"(p1), "s"(out), "s"(out2) : "memory");
            }
        }
        asm volatile(".Lcode_end0:");
        if (blockIdx.x == 0 && tid == 0) __hip_atomic_store((gu64*)(ws + WS_EP), tag, RLX_AGENT);
    }
}

extern "C" void kernel_launch(void* const* d_in, const int* in_sizes, int n_in, void* d_out, int out_size, void* d_ws,
                              size_t ws_size, hipStream_t stream) {
    (void)hipFuncSetAttribute((const void*)fused, hipFuncAttributeMaxDynamicSharedMemorySize, LDS_BYTES);
    fused<<<256, 512, LDS_BYTES, stream>>>((const u16*)d_in[0], (const u16*)d_in[1], (const u16*)d_in[2], (const u16*)d_in[4],
                                           (const u16*)d_in[5], (const u16*)d_in[6], (const u16*)d_in[7], (char*)d_ws, (u16*)d_out);
}
```
